```python
import torch

def _softmax(x, axis=-1):
    return torch.softmax(x, dim=axis)
def _gelu(x):
    return torch.nn.functional.gelu(x, approximate="tanh")
def _silu(x):
    return torch.nn.functional.silu(x)
def _sigmoid(x):
    return torch.sigmoid(x)
def _layernorm(x, axis=-1, eps=1e-5):
    m = x.mean(dim=axis, keepdim=True)
    v = x.var(dim=axis, keepdim=True, unbiased=False)
    return (x - m) / torch.sqrt(v + eps)
def _scatter_add(tgt, idx, src, axis=0):
    return tgt.clone().index_add_(axis, idx, src)
def _segment_sum(src, seg, n):
    out = torch.zeros((n,) + src.shape[1:], dtype=src.dtype)
    return out.index_add_(0, seg, src)
def _pad(x, pw):
    flat = tuple(v for pair in reversed(pw) for v in pair)
    return torch.nn.functional.pad(x, flat)

kernel_name = 'standalone__layernorm_linear__sampling'

def setup_inputs():

    _w = (lambda a: (lambda t: t.to(torch.bfloat16) if t.is_floating_point() else t)(torch.as_tensor(a)))
    B, S, D = 1024, 32, 448
    return {
        "x": _w((lambda *s: torch.randn(*s, dtype=torch.bfloat16) * 0.05)(B, S, D)),
        "g": _w((lambda *s: torch.randn(*s, dtype=torch.bfloat16) * 0.05)(D) + 1.0),
        "b": _w((lambda *s: torch.randn(*s, dtype=torch.bfloat16) * 0.4)(D)),
        "w": _w((lambda *s: torch.randn(*s, dtype=torch.bfloat16) * 0.05)(D, D)),
    }

def reference(x, g, b, w):
    return (_layernorm(x, axis=-1) * g + b) @ w
```

```cpp
#include <hip/hip_runtime.h>
#include <hip/hip_bf16.h>
#include <stdint.h>

typedef __attribute__((ext_vector_type(8))) short s8v;
typedef __attribute__((ext_vector_type(8))) __bf16 bf8v;
typedef __attribute__((ext_vector_type(4))) float f4v;

constexpr int D = 448;
constexpr int BM = 64;
constexpr int LDY = 456;

__device__ __forceinline__ float bf2f(unsigned short u) { return __uint_as_float(((unsigned)u) << 16); }
__device__ __forceinline__ unsigned short f2bf(float f) { __hip_bfloat16 h = __float2bfloat16(f); return *(unsigned short*)&h; }

__global__ __launch_bounds__(256) void ln_lin_naive(const unsigned short* __restrict__ x, const unsigned short* __restrict__ g,
                             const unsigned short* __restrict__ b, const unsigned short* __restrict__ w,
                             unsigned short* __restrict__ out, int M) {
    __shared__ __attribute__((aligned(16))) unsigned short ys[BM * LDY];
    int tid = threadIdx.x, lane = tid & 63, wave = tid >> 6;
    int row0 = blockIdx.x * BM;
    for (int r = wave; r < BM; r += 4) {
        const unsigned short* xr = x + (size_t)(row0 + r) * D;
        float v[7]; float s = 0.f;
        for (int i = 0; i < 7; i++) { v[i] = bf2f(xr[lane + 64 * i]); s += v[i]; }
        for (int o = 32; o > 0; o >>= 1) s += __shfl_xor(s, o);
        float m = s * (1.f / D); float q = 0.f;
        for (int i = 0; i < 7; i++) { float d = v[i] - m; q += d * d; }
        for (int o = 32; o > 0; o >>= 1) q += __shfl_xor(q, o);
        float rs = rsqrtf(q * (1.f / D) + 1e-5f);
        for (int i = 0; i < 7; i++) { int k = lane + 64 * i; ys[r * LDY + k] = f2bf((v[i] - m) * rs * bf2f(g[k]) + bf2f(b[k])); }
    }
    __syncthreads();
    f4v acc[4][7];
    for (int i = 0; i < 4; i++) for (int j = 0; j < 7; j++) acc[i][j] = (f4v){0, 0, 0, 0};
    int fr = lane & 15, fq = lane >> 4;
    for (int ks = 0; ks < 14; ks++) {
        s8v a[4];
        for (int i = 0; i < 4; i++) a[i] = *(const s8v*)&ys[(i * 16 + fr) * LDY + ks * 32 + fq * 8];
        for (int j = 0; j < 7; j++) {
            s8v bb;
            int n = wave * 112 + j * 16 + fr;
            for (int e = 0; e < 8; e++) bb[e] = (short)w[(size_t)(ks * 32 + fq * 8 + e) * D + n];
            for (int i = 0; i < 4; i++)
                acc[i][j] = __builtin_amdgcn_mfma_f32_16x16x32_bf16(__builtin_bit_cast(bf8v, a[i]), __builtin_bit_cast(bf8v, bb), acc[i][j], 0, 0, 0);
        }
    }
    for (int i = 0; i < 4; i++) for (int j = 0; j < 7; j++) for (int e = 0; e < 4; e++)
        out[(size_t)(row0 + i * 16 + fq * 4 + e) * D + wave * 112 + j * 16 + fr] = f2bf(acc[i][j][e]);
}

extern "C" void kernel_launch(void* const* d_in, const int* in_sizes, int n_in,
                              void* d_out, int out_size, void* d_ws, size_t ws_size,
                              hipStream_t stream) {
    const unsigned short* x = (const unsigned short*)d_in[0];
    const unsigned short* g = (const unsigned short*)d_in[1];
    const unsigned short* b = (const unsigned short*)d_in[2];
    const unsigned short* w = (const unsigned short*)d_in[3];
    int M = in_sizes[0] / D;
    ln_lin_naive<<<M / BM, 256, 0, stream>>>(x, g, b, w, (unsigned short*)d_out, M);
}
```

```cpp
#include <hip/hip_runtime.h>
#include <stdint.h>

extern "C" __global__ __launch_bounds__(256, 1) void ln_linear_kernel(const void* x, const void* g, const void* b, const void* w, void* out) {
    __shared__ __attribute__((aligned(1024))) char smem[163840];
    unsigned tid = threadIdx.x;
    asm volatile(
    "s_load_dwordx8 s[4:11], %0, 0\n\t"
    "s_load_dwordx2 s[12:13], %0, 32\n\t"
    "v_lshrrev_b32 v84, 6, %2\n\t"
    "v_lshrrev_b32 v85, 3, %2\n\t"
    "v_readfirstlane_b32 s36, v84\n\t"
    "s_getpc_b64 s[56:57]\n\t"
    "Lpc_%=:\n\t"
    "s_and_b32 s57, s57, 0xffff\n\t"
    "s_mov_b32 s58, Lend_%=-Lpc_%=\n\t"
    "s_mov_b32 s59, 0x20000\n\t"
    "s_bfe_u32 s37, %1, 0x50003\n\t"
    "v_lshrrev_b32 v87, 3, %2\n\t"
    "s_lshl_b32 s37, s37, 10\n\t"
    "v_lshlrev_b32 v87, 15, v87\n\t"
    "v_and_b32 v88, 7, %2\n\t"
    "v_lshl_add_u32 v87, v88, 7, v87\n\t"
    "buffer_load_dword a254, v87, s[56:59], s37 offen\n\t"
    "v_and_b32 v84, 6, v85\n\t"
    "v_and_b32 v86, 7, %2\n\t"
    "v_xor_b32 v84, v84, v86\n\t"
    "v_lshlrev_b32 v108, 4, v84\n\t"
    "s_movk_i32 s44, 896\n\t"
    "v_mad_u32_u24 v1, v85, s44, v108\n\t"
    "s_mul_i32 s42, s36, 7168\n\t"
    "s_add_u32 s42, s42, %3\n\t"
    "s_mov_b32 s18, 114688\n\t"
    "s_mov_b32 s19, 0x20000\n\t"
    "s_mul_i32 s45, %1, 114688\n\t"
    "s_mov_b32 s64, 0\n\t"
    "s_mov_b32 s66, 28672\n\t"
    "v_and_b32 v19, 63, %2\n\t"
    "s_mul_i32 s43, s36, 12288\n\t"
    "s_add_u32 s43, s43, %3\n\t"
    "s_add_u32 s43, s43, 114688\n\t"
    "s_movk_i32 s30, 896\n\t"
    "s_mov_b32 s31, 0x20000\n\t"
    "s_movk_i32 s54, 896\n\t"
    "s_mov_b32 s55, 0x20000\n\t"
    "s_mov_b32 s22, 401408\n\t"
    "s_mov_b32 s23, 0x20000\n\t"
    "s_mov_b32 s65, 14336\n\t"
    "s_mov_b32 s67, 43008\n\t"
    "s_mov_b32 s68, 57344\n\t"
    "s_mov_b32 s69, 71680\n\t"
    "s_mov_b32 s70, 86016\n\t"
    "s_mov_b32 s71, 100352\n\t"
    "s_mov_b32 s72, 114688\n\t"
    "s_mov_b32 s73, 129024\n\t"
    "s_mov_b32 s74, 143360\n\t"
    "s_mov_b32 s75, 157696\n\t"
    "s_mov_b32 s76, 172032\n\t"
    "s_mov_b32 s77, 186368\n\t"
    "s_mov_b32 s78, 200704\n\t"
    "s_mov_b32 s79, 215040\n\t"
    "s_mov_b32 s80, 229376\n\t"
    "s_mov_b32 s81, 243712\n\t"
    "s_mov_b32 s82, 258048\n\t"
    "s_mov_b32 s83, 272384\n\t"
    "s_mov_b32 s84, 286720\n\t"
    "s_mov_b32 s85, 301056\n\t"
    "s_mov_b32 s86, 315392\n\t"
    "s_mov_b32 s87, 329728\n\t"
    "s_mov_b32 s88, 344064\n\t"
    "s_mov_b32 s89, 358400\n\t"
    "s_mov_b32 s90, 372736\n\t"
    "s_mov_b32 s91, 387072\n\t"
    "s_mov_b32 s26, 114688\n\t"
    "s_mov_b32 s27, 0x20000\n\t"
    "s_mov_b32 s38, 0x3f803f80\n\t"
    "s_mov_b32 s39, 0xffff0000\n\t"
    "s_mov_b32 s40, 0x3b124925\n\t"
    "s_mov_b32 s41, 0x3727c5ac\n\t"
    "s_mov_b32 s46, 0x55555555\n\t"
    "s_mov_b32 s47, s46\n\t"
    "s_not_b64 s[34:35], s[46:47]\n\t"
    "v_lshl_add_u32 v3, v19, 4, s42\n\t"
    "v_add_u32 v4, 57344, v3\n\t"
    "v_add_u32 v108, s43, v108\n\t"
    "s_movk_i32 s37, 896\n\t"
    "s_lshl_b32 s60, s36, 6\n\t"
    "v_lshrrev_b32 v84, 5, v19\n\t"
    "v_and_b32 v85, 3, v19\n\t"
    "v_xor_b32 v86, v84, v85\n\t"
    "v_lshl_add_u32 v86, v86, 4, s60\n\t"
    "v_lshrrev_b32 v87, 2, v19\n\t"
    "v_mad_u32_u24 v2, v87, s37, v86\n\t"
    "v_and_b32 v86, 14, v19\n\t"
    "v_bfe_u32 v88, v19, 4, 1\n\t"
    "v_or_b32 v86, v86, v88\n\t"
    "v_and_b32 v89, 1, v19\n\t"
    "v_lshl_add_u32 v84, v84, 1, v89\n\t"
    "v_lshl_add_u32 v84, v84, 4, s60\n\t"
    "v_mad_u32_u24 v16, v86, s37, v84\n\t"
    "v_lshrrev_b32 v90, 4, v19\n\t"
    "v_bfe_u32 v86, v19, 2, 2\n\t"
    "v_lshl_add_u32 v86, v90, 3, v86\n\t"
    "v_bfe_u32 v87, v19, 1, 1\n\t"
    "v_lshl_add_u32 v86, v86, 1, v87\n\t"
    "v_lshl_add_u32 v86, v86, 1, v88\n\t"
    "v_lshl_add_u32 v86, v86, 1, v89\n\t"
    "v_lshl_add_u32 v5, v86, 3, s43\n\t"
    "v_xor_b32 v6, 16, v5\n\t"
    "v_and_b32 v86, 6, v19\n\t"
    "v_xor_b32 v86, v86, v90\n\t"
    "v_and_b32 v87, 7, v19\n\t"
    "v_lshl_add_u32 v86, v87, 3, v86\n\t"
    "v_lshl_add_u32 v86, v86, 4, %3\n\t"
    "v_bfe_u32 v87, v19, 3, 1\n\t"
    "s_movk_i32 s37, 7168\n\t"
    "v_mad_u32_u24 v86, v87, s37, v86\n\t"
    "s_lshr_b32 s37, s36, 1\n\t"
    "s_mul_i32 s62, s37, 14336\n\t"
    "s_lshl_b32 s37, s37, 7\n\t"
    "s_sub_u32 s60, 256, s37\n\t"
    "s_add_u32 s61, s60, 512\n\t"
    "s_sub_u32 s37, 14336, s62\n\t"
    "v_add_u32 v8, s62, v86\n\t"
    "v_add_u32 v10, s37, v86\n\t"
    "v_xor_b32 v9, 64, v8\n\t"
    "v_xor_b32 v11, 64, v10\n\t"
    "v_add_u32 v12, 57344, v8\n\t"
    "v_add_u32 v13, 57344, v9\n\t"
    "v_add_u32 v14, 57344, v10\n\t"
    "v_add_u32 v15, 57344, v11\n\t"
    "v_add_u32 v17, s37, v16\n\t"
    "v_add_u32 v16, s62, v16\n\t"
    "v_add_u32 v18, s61, v16\n\t"
    "v_lshlrev_b32 v88, 4, v19\n\t"
    "s_waitcnt lgkmcnt(0)\n\t"
    "s_add_u32 s16, s4, s45\n\t"
    "s_addc_u32 s17, s5, 0\n\t"
    "s_and_b32 s17, s17, 0xffff\n\t"
    "s_mov_b32 s20, s10\n\t"
    "s_and_b32 s21, s11, 0xffff\n\t"
    "s_add_u32 m0, s42, 0\n\t"
    "s_nop 0\n\t"
    "buffer_load_dwordx4 v1, s[16:19], s64 offen offset:0 nt lds\n\t"
    "s_add_u32 m0, s42, 896\n\t"
    "s_nop 0\n\t"
    "buffer_load_dwordx4 v1, s[16:19], s64 offen offset:128 nt lds\n\t"
    "s_add_u32 m0, s42, 1792\n\t"
    "s_nop 0\n\t"
    "buffer_load_dwordx4 v1, s[16:19], s64 offen offset:256 nt lds\n\t"
    "s_add_u32 m0, s42, 2688\n\t"
    "s_nop 0\n\t"
    "buffer_load_dwordx4 v1, s[16:19], s64 offen offset:384 nt lds\n\t"
    "s_add_u32 m0, s42, 3584\n\t"
    "s_nop 0\n\t"
    "buffer_load_dwordx4 v1, s[16:19], s64 offen offset:512 nt lds\n\t"
    "s_add_u32 m0, s42, 4480\n\t"
    "s_nop 0\n\t"
    "buffer_load_dwordx4 v1, s[16:19], s64 offen offset:640 nt lds\n\t"
    "s_add_u32 m0, s42, 5376\n\t"
    "s_nop 0\n\t"
    "buffer_load_dwordx4 v1, s[16:19], s64 offen offset:768 nt lds\n\t"
    "s_add_u32 m0, s43, 0\n\t"
    "s_mov_b32 s28, s6\n\t"
    "s_and_b32 s29, s7, 0xffff\n\t"
    "buffer_load_dwordx4 v88, s[28:31], 0 offen  lds\n\t"
    "s_add_u32 m0, s43, 1024\n\t"
    "s_mov_b32 s52, s8\n\t"
    "s_and_b32 s53, s9, 0xffff\n\t"
    "buffer_load_dwordx4 v88, s[52:55], 0 offen  lds\n\t"
    "s_add_u32 s24, s12, s45\n\t"
    "s_addc_u32 s25, s13, 0\n\t"
    "s_and_b32 s25, s25, 0xffff\n\t"
    "s_bfe_u32 s37, %1, 0x50003\n\t"
    "s_mul_i32 s37, s37, 12544\n\t"
    "s_lshr_b32 s63, s36, 1\n\t"
    "s_mul_i32 s63, s63, 6272\n\t"
    "s_add_u32 s37, s37, s63\n\t"
    "s_and_b32 s63, s36, 1\n\t"
    "s_mul_i32 s63, s63, 128\n\t"
    "s_add_u32 s37, s37, s63\n\t"
    "s_add_u32 s92, s10, s37\n\t"
    "s_addc_u32 s93, s11, 0\n\t"
    "s_load_dword s94, s[92:93], 0\n\t"
    "s_load_dword s94, s[92:93], 64\n\t"
    "s_load_dword s94, s[92:93], 896\n\t"
    "s_load_dword s94, s[92:93], 960\n\t"
    "s_load_dword s94, s[92:93], 1792\n\t"
    "s_load_dword s94, s[92:93], 1856\n\t"
    "s_load_dword s94, s[92:93], 2688\n\t"
    "s_load_dword s94, s[92:93], 2752\n\t"
    "s_load_dword s94, s[92:93], 3584\n\t"
    "s_load_dword s94, s[92:93], 3648\n\t"
    "s_load_dword s94, s[92:93], 4480\n\t"
    "s_load_dword s94, s[92:93], 4544\n\t"
    "s_load_dword s94, s[92:93], 5376\n\t"
    "s_load_dword s94, s[92:93], 5440\n\t"
    "s_bfe_u32 s37, %1, 0x50003\n\t"
    "s_mul_i32 s37, s37, 12544\n\t"
    "v_cmp_gt_u32 vcc, 70, %2\n\t"
    "v_mul_u32_u24 v93, 205, %2\n\t"
    "v_lshrrev_b32 v93, 10, v93\n\t"
    "v_mul_u32_u24 v93, 2, v93\n\t"
    "v_add3_u32 v92, v93, %2, 2\n\t"
    "v_lshlrev_b32 v92, 7, v92\n\t"
    "v_bfrev_b32 v94, -2\n\t"
    "v_add_u32 v92, s37, v92\n\t"
    "v_cndmask_b32 v255, v94, v92, vcc\n\t"
    "s_add_u32 m0, s42, 28672\n\t"
    "s_nop 0\n\t"
    "buffer_load_dwordx4 v1, s[16:19], s66 offen offset:0 nt lds\n\t"
    "s_waitcnt vmcnt(9)\n\t"
    "s_add_u32 m0, s42, 29568\n\t"
    "s_nop 0\n\t"
    "buffer_load_dwordx4 v1, s[16:19], s66 offen offset:128 nt lds\n\t"
    "s_waitcnt vmcnt(9)\n\t"
    "s_add_u32 m0, s42, 30464\n\t"
    "s_nop 0\n\t"
    "buffer_load_dwordx4 v1, s[16:19], s66 offen offset:256 nt lds\n\t"
    "s_waitcnt vmcnt(9)\n\t"
    "s_add_u32 m0, s42, 31360\n\t"
    "s_nop 0\n\t"
    "buffer_load_dwordx4 v1, s[16:19], s66 offen offset:384 nt lds\n\t"
    "s_waitcnt vmcnt(9)\n\t"
    "s_add_u32 m0, s42, 32256\n\t"
    "s_nop 0\n\t"
    "buffer_load_dwordx4 v1, s[16:19], s66 offen offset:512 nt lds\n\t"
    "s_waitcnt vmcnt(9)\n\t"
    "s_add_u32 m0, s42, 33152\n\t"
    "s_nop 0\n\t"
    "buffer_load_dwordx4 v1, s[16:19], s66 offen offset:640 nt lds\n\t"
    "s_waitcnt vmcnt(9)\n\t"
    "s_add_u32 m0, s42, 34048\n\t"
    "s_nop 0\n\t"
    "buffer_load_dwordx4 v1, s[16:19], s66 offen offset:768 nt lds\n\t"
    "s_waitcnt vmcnt(9)\n\t"
    "s_add_u32 m0, s42, 57344\n\t"
    "s_nop 0\n\t"
    "buffer_load_dwordx4 v1, s[16:19], s68 offen offset:0 nt lds\n\t"
    "s_waitcnt lgkmcnt(0)\n\t"
    "s_waitcnt vmcnt(9)\n\t"
    "ds_read_b128 v[164:167], v108 offset:0\n\t"
    "ds_read_b128 v[168:171], v108 offset:128\n\t"
    "ds_read_b128 v[172:175], v108 offset:256\n\t"
    "ds_read_b128 v[176:179], v108 offset:384\n\t"
    "ds_read_b128 v[180:183], v108 offset:512\n\t"
    "ds_read_b128 v[184:187], v108 offset:640\n\t"
    "ds_read_b128 v[188:191], v108 offset:768\n\t"
    "s_waitcnt vmcnt(8)\n\t"
    "ds_read_b128 v[220:223], v108 offset:1024\n\t"
    "ds_read_b128 v[224:227], v108 offset:1152\n\t"
    "ds_read_b128 v[228:231], v108 offset:1280\n\t"
    "ds_read_b128 v[232:235], v108 offset:1408\n\t"
    "ds_read_b128 v[236:239], v108 offset:1536\n\t"
    "ds_read_b128 v[240:243], v108 offset:1664\n\t"
    "ds_read_b128 v[244:247], v108 offset:1792\n\t"
    "s_waitcnt lgkmcnt(13)\n\t"
    "v_lshlrev_b32 v136, 16, v164\n\t"
    "v_and_b32 v137, s39, v164\n\t"
    "v_lshlrev_b32 v138, 16, v165\n\t"
    "v_and_b32 v139, s39, v165\n\t"
    "v_lshlrev_b32 v140, 16, v166\n\t"
    "v_and_b32 v141, s39, v166\n\t"
    "v_lshlrev_b32 v142, 16, v167\n\t"
    "v_and_b32 v143, s39, v167\n\t"
    "s_waitcnt lgkmcnt(12)\n\t"
    "v_lshlrev_b32 v144, 16, v168\n\t"
    "v_and_b32 v145, s39, v168\n\t"
    "v_lshlrev_b32 v146, 16, v169\n\t"
    "v_and_b32 v147, s39, v169\n\t"
    "v_lshlrev_b32 v148, 16, v170\n\t"
    "v_and_b32 v149, s39, v170\n\t"
    "v_lshlrev_b32 v150, 16, v171\n\t"
    "v_and_b32 v151, s39, v171\n\t"
    "s_waitcnt lgkmcnt(11)\n\t"
    "v_lshlrev_b32 v152, 16, v172\n\t"
    "v_and_b32 v153, s39, v172\n\t"
    "v_lshlrev_b32 v154, 16, v173\n\t"
    "v_and_b32 v155, s39, v173\n\t"
    "v_lshlrev_b32 v156, 16, v174\n\t"
    "v_and_b32 v157, s39, v174\n\t"
    "v_lshlrev_b32 v158, 16, v175\n\t"
    "v_and_b32 v159, s39, v175\n\t"
    "s_waitcnt lgkmcnt(10)\n\t"
    "v_lshlrev_b32 v160, 16, v176\n\t"
    "v_and_b32 v161, s39, v176\n\t"
    "v_lshlrev_b32 v162, 16, v177\n\t"
    "v_and_b32 v163, s39, v177\n\t"
    "v_lshlrev_b32 v164, 16, v178\n\t"
    "v_and_b32 v165, s39, v178\n\t"
    "v_lshlrev_b32 v166, 16, v179\n\t"
    "v_and_b32 v167, s39, v179\n\t"
    "s_waitcnt lgkmcnt(9)\n\t"
    "v_lshlrev_b32 v168, 16, v180\n\t"
    "v_and_b32 v169, s39, v180\n\t"
    "v_lshlrev_b32 v170, 16, v181\n\t"
    "v_and_b32 v171, s39, v181\n\t"
    "v_lshlrev_b32 v172, 16, v182\n\t"
    "v_and_b32 v173, s39, v182\n\t"
    "v_lshlrev_b32 v174, 16, v183\n\t"
    "v_and_b32 v175, s39, v183\n\t"
    "s_waitcnt lgkmcnt(8)\n\t"
    "v_lshlrev_b32 v176, 16, v184\n\t"
    "v_and_b32 v177, s39, v184\n\t"
    "v_lshlrev_b32 v178, 16, v185\n\t"
    "v_and_b32 v179, s39, v185\n\t"
    "v_lshlrev_b32 v180, 16, v186\n\t"
    "v_and_b32 v181, s39, v186\n\t"
    "v_lshlrev_b32 v182, 16, v187\n\t"
    "v_and_b32 v183, s39, v187\n\t"
    "s_waitcnt lgkmcnt(7)\n\t"
    "v_lshlrev_b32 v184, 16, v188\n\t"
    "v_and_b32 v185, s39, v188\n\t"
    "v_lshlrev_b32 v186, 16, v189\n\t"
    "v_and_b32 v187, s39, v189\n\t"
    "v_lshlrev_b32 v188, 16, v190\n\t"
    "v_and_b32 v189, s39, v190\n\t"
    "v_lshlrev_b32 v190, 16, v191\n\t"
    "v_and_b32 v191, s39, v191\n\t"
    "s_waitcnt lgkmcnt(6)\n\t"
    "v_lshlrev_b32 v192, 16, v220\n\t"
    "v_and_b32 v193, s39, v220\n\t"
    "v_lshlrev_b32 v194, 16, v221\n\t"
    "v_and_b32 v195, s39, v221\n\t"
    "v_lshlrev_b32 v196, 16, v222\n\t"
    "v_and_b32 v197, s39, v222\n\t"
    "v_lshlrev_b32 v198, 16, v223\n\t"
    "v_and_b32 v199, s39, v223\n\t"
    "s_waitcnt lgkmcnt(5)\n\t"
    "v_lshlrev_b32 v200, 16, v224\n\t"
    "v_and_b32 v201, s39, v224\n\t"
    "v_lshlrev_b32 v202, 16, v225\n\t"
    "v_and_b32 v203, s39, v225\n\t"
    "v_lshlrev_b32 v204, 16, v226\n\t"
    "v_and_b32 v205, s39, v226\n\t"
    "v_lshlrev_b32 v206, 16, v227\n\t"
    "v_and_b32 v207, s39, v227\n\t"
    "s_waitcnt lgkmcnt(4)\n\t"
    "v_lshlrev_b32 v208, 16, v228\n\t"
    "v_and_b32 v209, s39, v228\n\t"
    "v_lshlrev_b32 v210, 16, v229\n\t"
    "v_and_b32 v211, s39, v229\n\t"
    "v_lshlrev_b32 v212, 16, v230\n\t"
    "v_and_b32 v213, s39, v230\n\t"
    "v_lshlrev_b32 v214, 16, v231\n\t"
    "v_and_b32 v215, s39, v231\n\t"
    "s_waitcnt lgkmcnt(3)\n\t"
    "v_lshlrev_b32 v216, 16, v232\n\t"
    "v_and_b32 v217, s39, v232\n\t"
    "v_lshlrev_b32 v218, 16, v233\n\t"
    "v_and_b32 v219, s39, v233\n\t"
    "v_lshlrev_b32 v220, 16, v234\n\t"
    "v_and_b32 v221, s39, v234\n\t"
    "v_lshlrev_b32 v222, 16, v235\n\t"
    "v_and_b32 v223, s39, v235\n\t"
    "s_waitcnt lgkmcnt(2)\n\t"
    "v_lshlrev_b32 v224, 16, v236\n\t"
    "v_and_b32 v225, s39, v236\n\t"
    "v_lshlrev_b32 v226, 16, v237\n\t"
    "v_and_b32 v227, s39, v237\n\t"
    "v_lshlrev_b32 v228, 16, v238\n\t"
    "v_and_b32 v229, s39, v238\n\t"
    "v_lshlrev_b32 v230, 16, v239\n\t"
    "v_and_b32 v231, s39, v239\n\t"
    "s_waitcnt lgkmcnt(1)\n\t"
    "v_lshlrev_b32 v232, 16, v240\n\t"
    "v_and_b32 v233, s39, v240\n\t"
    "v_lshlrev_b32 v234, 16, v241\n\t"
    "v_and_b32 v235, s39, v241\n\t"
    "v_lshlrev_b32 v236, 16, v242\n\t"
    "v_and_b32 v237, s39, v242\n\t"
    "v_lshlrev_b32 v238, 16, v243\n\t"
    "v_and_b32 v239, s39, v243\n\t"
    "s_waitcnt lgkmcnt(0)\n\t"
    "v_lshlrev_b32 v240, 16, v244\n\t"
    "v_and_b32 v241, s39, v244\n\t"
    "v_lshlrev_b32 v242, 16, v245\n\t"
    "v_and_b32 v243, s39, v245\n\t"
    "v_lshlrev_b32 v244, 16, v246\n\t"
    "v_and_b32 v245, s39, v246\n\t"
    "v_lshlrev_b32 v246, 16, v247\n\t"
    "v_and_b32 v247, s39, v247\n\t"
    "ds_read_b128 v[56:59], v3 offset:0\n\t"
    "ds_read_b128 v[60:63], v3 offset:1024\n\t"
    "ds_read_b128 v[64:67], v3 offset:2048\n\t"
    "ds_read_b128 v[68:71], v3 offset:3072\n\t"
    "ds_read_b128 v[72:75], v3 offset:4096\n\t"
    "ds_read_b128 v[76:79], v3 offset:5120\n\t"
    "ds_read_b128 v[80:83], v3 offset:6144\n\t"
    "s_waitcnt lgkmcnt(6)\n\t"
    "v_dot2_f32_bf16 v100, v56, s38, 0\n\t"
    "s_add_u32 m0, s42, 58240\n\t"
    "v_dot2_f32_bf16 v102, v56, v56, 0\n\t"
    "v_dot2_f32_bf16 v101, v57, s38, 0\n\t"
    "buffer_load_dwordx4 v1, s[16:19], s68 offen offset:128 nt lds\n\t"
    "v_dot2_f32_bf16 v103, v57, v57, 0\n\t"
    "v_dot2_f32_bf16 v100, v58, s38, v100\n\t"
    "v_dot2_f32_bf16 v102, v58, v58, v102\n\t"
    "v_dot2_f32_bf16 v101, v59, s38, v101\n\t"
    "v_dot2_f32_bf16 v103, v59, v59, v103\n\t"
    "s_waitcnt lgkmcnt(5)\n\t"
    "v_dot2_f32_bf16 v100, v60, s38, v100\n\t"
    "v_dot2_f32_bf16 v102, v60, v60, v102\n\t"
    "v_dot2_f32_bf16 v101, v61, s38, v101\n\t"
    "v_dot2_f32_bf16 v103, v61, v61, v103\n\t"
    "v_dot2_f32_bf16 v100, v62, s38, v100\n\t"
    "v_dot2_f32_bf16 v102, v62, v62, v102\n\t"
    "v_dot2_f32_bf16 v101, v63, s38, v101\n\t"
    "v_dot2_f32_bf16 v103, v63, v63, v103\n\t"
    "s_waitcnt lgkmcnt(4)\n\t"
    "v_dot2_f32_bf16 v100, v64, s38, v100\n\t"
    "v_dot2_f32_bf16 v102, v64, v64, v102\n\t"
    "v_dot2_f32_bf16 v101, v65, s38, v101\n\t"
    "v_dot2_f32_bf16 v103, v65, v65, v103\n\t"
    "v_dot2_f32_bf16 v100, v66, s38, v100\n\t"
    "v_dot2_f32_bf16 v102, v66, v66, v102\n\t"
    "v_dot2_f32_bf16 v101, v67, s38, v101\n\t"
    "v_dot2_f32_bf16 v103, v67, v67, v103\n\t"
    "s_waitcnt lgkmcnt(3)\n\t"
    "v_dot2_f32_bf16 v100, v68, s38, v100\n\t"
    "v_dot2_f32_bf16 v102, v68, v68, v102\n\t"
    "v_dot2_f32_bf16 v101, v69, s38, v101\n\t"
    "v_dot2_f32_bf16 v103, v69, v69, v103\n\t"
    "v_dot2_f32_bf16 v100, v70, s38, v100\n\t"
    "v_dot2_f32_bf16 v102, v70, v70, v102\n\t"
    "v_dot2_f32_bf16 v101, v71, s38, v101\n\t"
    "v_dot2_f32_bf16 v103, v71, v71, v103\n\t"
    "s_waitcnt lgkmcnt(2)\n\t"
    "v_dot2_f32_bf16 v100, v72, s38, v100\n\t"
    "v_dot2_f32_bf16 v102, v72, v72, v102\n\t"
    "v_dot2_f32_bf16 v101, v73, s38, v101\n\t"
    "v_dot2_f32_bf16 v103, v73, v73, v103\n\t"
    "v_dot2_f32_bf16 v100, v74, s38, v100\n\t"
    "v_dot2_f32_bf16 v102, v74, v74, v102\n\t"
    "v_dot2_f32_bf16 v101, v75, s38, v101\n\t"
    "v_dot2_f32_bf16 v103, v75, v75, v103\n\t"
    "s_waitcnt lgkmcnt(1)\n\t"
    "v_dot2_f32_bf16 v100, v76, s38, v100\n\t"
    "s_add_u32 m0, s42, 59136\n\t"
    "v_dot2_f32_bf16 v102, v76, v76, v102\n\t"
    "v_dot2_f32_bf16 v101, v77, s38, v101\n\t"
    "buffer_load_dwordx4 v1, s[16:19], s68 offen offset:256 nt lds\n\t"
    "v_dot2_f32_bf16 v103, v77, v77, v103\n\t"
    "v_dot2_f32_bf16 v100, v78, s38, v100\n\t"
    "v_dot2_f32_bf16 v102, v78, v78, v102\n\t"
    "v_dot2_f32_bf16 v101, v79, s38, v101\n\t"
    "v_dot2_f32_bf16 v103, v79, v79, v103\n\t"
    "s_waitcnt lgkmcnt(0)\n\t"
    "v_dot2_f32_bf16 v100, v80, s38, v100\n\t"
    "v_dot2_f32_bf16 v102, v80, v80, v102\n\t"
    "v_dot2_f32_bf16 v101, v81, s38, v101\n\t"
    "v_dot2_f32_bf16 v103, v81, v81, v103\n\t"
    "v_dot2_f32_bf16 v100, v82, s38, v100\n\t"
    "v_dot2_f32_bf16 v102, v82, v82, v102\n\t"
    "v_dot2_f32_bf16 v101, v83, s38, v101\n\t"
    "v_dot2_f32_bf16 v103, v83, v83, v103\n\t"
    "v_lshlrev_b32 v84, 16, v56\n\t"
    "v_and_b32 v85, s39, v56\n\t"
    "v_lshlrev_b32 v86, 16, v57\n\t"
    "v_add_f32 v100, v100, v101\n\t"
    "v_add_f32 v102, v102, v103\n\t"
    "v_and_b32 v87, s39, v57\n\t"
    "v_add_f32_dpp v100, v100, v100 quad_perm:[1,0,3,2] row_mask:0xf bank_mask:0xf\n\t"
    "v_add_f32_dpp v102, v102, v102 quad_perm:[1,0,3,2] row_mask:0xf bank_mask:0xf\n\t"
    "v_lshlrev_b32 v88, 16, v58\n\t"
    "v_add_f32_dpp v100, v100, v100 quad_perm:[2,3,0,1] row_mask:0xf bank_mask:0xf\n\t"
    "v_add_f32_dpp v102, v102, v102 quad_perm:[2,3,0,1] row_mask:0xf bank_mask:0xf\n\t"
    "v_and_b32 v89, s39, v58\n\t"
    "v_add_f32_dpp v100, v100, v100 row_half_mirror row_mask:0xf bank_mask:0xf\n\t"
    "v_add_f32_dpp v102, v102, v102 row_half_mirror row_mask:0xf bank_mask:0xf\n\t"
    "v_mul_f32 v101, s40, v100\n\t"
    "v_mul_f32 v103, s40, v102\n\t"
    "v_lshlrev_b32 v90, 16, v59\n\t"
    "v_fma_f32 v103, -v101, v101, v103\n\t"
    "v_and_b32 v91, s39, v59\n\t"
    "v_max_f32 v103, 0, v103\n\t"
    "v_lshlrev_b32 v92, 16, v60\n\t"
    "v_add_f32 v103, s41, v103\n\t"
    "v_and_b32 v93, s39, v60\n\t"
    "v_rsq_f32 v104, v103\n\t"
    "v_lshlrev_b32 v94, 16, v61\n\t"
    "s_add_u32 m0, s42, 60032\n\t"
    "v_and_b32 v95, s39, v61\n\t"
    "v_lshlrev_b32 v96, 16, v62\n\t"
    "buffer_load_dwordx4 v1, s[16:19], s68 offen offset:384 nt lds\n\t"
    "v_mul_f32_e64 v105, -v101, v104\n\t"
    "v_and_b32 v97, s39, v62\n\t"
    "v_lshlrev_b32 v98, 16, v63\n\t"
    "v_and_b32 v99, s39, v63\n\t"
    "v_pk_fma_f32 v[84:85], v[84:85], v[104:105], v[104:105] op_sel:[0,0,1] op_sel_hi:[1,0,1]\n\t"
    "v_pk_fma_f32 v[86:87], v[86:87], v[104:105], v[104:105] op_sel:[0,0,1] op_sel_hi:[1,0,1]\n\t"
    "v_pk_fma_f32 v[88:89], v[88:89], v[104:105], v[104:105] op_sel:[0,0,1] op_sel_hi:[1,0,1]\n\t"
    "v_pk_fma_f32 v[90:91], v[90:91], v[104:105], v[104:105] op_sel:[0,0,1] op_sel_hi:[1,0,1]\n\t"
    "v_pk_fma_f32 v[84:85], v[84:85], v[136:137], v[192:193]\n\t"
    "v_pk_fma_f32 v[86:87], v[86:87], v[138:139], v[194:195]\n\t"
    "v_pk_fma_f32 v[88:89], v[88:89], v[140:141], v[196:197]\n\t"
    "v_pk_fma_f32 v[90:91], v[90:91], v[142:143], v[198:199]\n\t"
    "v_cvt_pk_bf16_f32 v56, v84, v85\n\t"
    "v_cvt_pk_bf16_f32 v57, v86, v87\n\t"
    "v_cvt_pk_bf16_f32 v58, v88, v89\n\t"
    "v_cvt_pk_bf16_f32 v59, v90, v91\n\t"
    "ds_write_b128 v3, v[56:59] offset:0\n\t"
    "v_lshlrev_b32 v84, 16, v64\n\t"
    "v_and_b32 v85, s39, v64\n\t"
    "v_lshlrev_b32 v86, 16, v65\n\t"
    "v_and_b32 v87, s39, v65\n\t"
    "v_lshlrev_b32 v88, 16, v66\n\t"
    "v_and_b32 v89, s39, v66\n\t"
    "v_lshlrev_b32 v90, 16, v67\n\t"
    "v_and_b32 v91, s39, v67\n\t"
    "v_pk_fma_f32 v[92:93], v[92:93], v[104:105], v[104:105] op_sel:[0,0,1] op_sel_hi:[1,0,1]\n\t"
    "v_pk_fma_f32 v[94:95], v[94:95], v[104:105], v[104:105] op_sel:[0,0,1] op_sel_hi:[1,0,1]\n\t"
    "v_pk_fma_f32 v[96:97], v[96:97], v[104:105], v[104:105] op_sel:[0,0,1] op_sel_hi:[1,0,1]\n\t"
    "v_pk_fma_f32 v[98:99], v[98:99], v[104:105], v[104:105] op_sel:[0,0,1] op_sel_hi:[1,0,1]\n\t"
    "v_pk_fma_f32 v[92:93], v[92:93], v[144:145], v[200:201]\n\t"
    "v_pk_fma_f32 v[94:95], v[94:95], v[146:147], v[202:203]\n\t"
    "v_pk_fma_f32 v[96:97], v[96:97], v[148:149], v[204:205]\n\t"
    "v_pk_fma_f32 v[98:99], v[98:99], v[150:151], v[206:207]\n\t"
    "v_cvt_pk_bf16_f32 v60, v92, v93\n\t"
    "v_cvt_pk_bf16_f32 v61, v94, v95\n\t"
    "v_cvt_pk_bf16_f32 v62, v96, v97\n\t"
    "v_cvt_pk_bf16_f32 v63, v98, v99\n\t"
    "ds_write_b128 v3, v[60:63] offset:1024\n\t"
    "s_add_u32 m0, s42, 60928\n\t"
    "v_lshlrev_b32 v92, 16, v68\n\t"
    "v_and_b32 v93, s39, v68\n\t"
    "buffer_load_dwordx4 v1, s[16:19], s68 offen offset:512 nt lds\n\t"
    "v_lshlrev_b32 v94, 16, v69\n\t"
    "v_and_b32 v95, s39, v69\n\t"
    "v_lshlrev_b32 v96, 16, v70\n\t"
    "v_and_b32 v97, s39, v70\n\t"
    "v_lshlrev_b32 v98, 16, v71\n\t"
    "v_and_b32 v99, s39, v71\n\t"
    "v_pk_fma_f32 v[84:85], v[84:85], v[104:105], v[104:105] op_sel:[0,0,1] op_sel_hi:[1,0,1]\n\t"
    "v_pk_fma_f32 v[86:87], v[86:87], v[104:105], v[104:105] op_sel:[0,0,1] op_sel_hi:[1,0,1]\n\t"
    "v_pk_fma_f32 v[88:89], v[88:89], v[104:105], v[104:105] op_sel:[0,0,1] op_sel_hi:[1,0,1]\n\t"
    "v_pk_fma_f32 v[90:91], v[90:91], v[104:105], v[104:105] op_sel:[0,0,1] op_sel_hi:[1,0,1]\n\t"
    "v_pk_fma_f32 v[84:85], v[84:85], v[152:153], v[208:209]\n\t"
    "v_pk_fma_f32 v[86:87], v[86:87], v[154:155], v[210:211]\n\t"
    "v_pk_fma_f32 v[88:89], v[88:89], v[156:157], v[212:213]\n\t"
    "v_pk_fma_f32 v[90:91], v[90:91], v[158:159], v[214:215]\n\t"
    "v_cvt_pk_bf16_f32 v64, v84, v85\n\t"
    "v_cvt_pk_bf16_f32 v65, v86, v87\n\t"
    "v_cvt_pk_bf16_f32 v66, v88, v89\n\t"
    "v_cvt_pk_bf16_f32 v67, v90, v91\n\t"
    "ds_write_b128 v3, v[64:67] offset:2048\n\t"
    "v_lshlrev_b32 v84, 16, v72\n\t"
    "v_and_b32 v85, s39, v72\n\t"
    "v_lshlrev_b32 v86, 16, v73\n\t"
    "v_and_b32 v87, s39, v73\n\t"
    "v_lshlrev_b32 v88, 16, v74\n\t"
    "v_and_b32 v89, s39, v74\n\t"
    "v_lshlrev_b32 v90, 16, v75\n\t"
    "v_and_b32 v91, s39, v75\n\t"
    "v_pk_fma_f32 v[92:93], v[92:93], v[104:105], v[104:105] op_sel:[0,0,1] op_sel_hi:[1,0,1]\n\t"
    "v_pk_fma_f32 v[94:95], v[94:95], v[104:105], v[104:105] op_sel:[0,0,1] op_sel_hi:[1,0,1]\n\t"
    "v_pk_fma_f32 v[96:97], v[96:97], v[104:105], v[104:105] op_sel:[0,0,1] op_sel_hi:[1,0,1]\n\t"
    "v_pk_fma_f32 v[98:99], v[98:99], v[104:105], v[104:105] op_sel:[0,0,1] op_sel_hi:[1,0,1]\n\t"
    "v_pk_fma_f32 v[92:93], v[92:93], v[160:161], v[216:217]\n\t"
    "v_pk_fma_f32 v[94:95], v[94:95], v[162:163], v[218:219]\n\t"
    "v_pk_fma_f32 v[96:97], v[96:97], v[164:165], v[220:221]\n\t"
    "v_pk_fma_f32 v[98:99], v[98:99], v[166:167], v[222:223]\n\t"
    "v_cvt_pk_bf16_f32 v68, v92, v93\n\t"
    "v_cvt_pk_bf16_f32 v69, v94, v95\n\t"
    "v_cvt_pk_bf16_f32 v70, v96, v97\n\t"
    "s_add_u32 m0, s42, 61824\n\t"
    "v_cvt_pk_bf16_f32 v71, v98, v99\n\t"
    "ds_write_b128 v3, v[68:71] offset:3072\n\t"
    "buffer_load_dwordx4 v1, s[16:19], s68 offen offset:640 nt lds\n\t"
    "v_lshlrev_b32 v92, 16, v76\n\t"
    "v_and_b32 v93, s39, v76\n\t"
    "v_lshlrev_b32 v94, 16, v77\n\t"
    "v_and_b32 v95, s39, v77\n\t"
    "v_lshlrev_b32 v96, 16, v78\n\t"
    "v_and_b32 v97, s39, v78\n\t"
    "v_lshlrev_b32 v98, 16, v79\n\t"
    "v_and_b32 v99, s39, v79\n\t"
    "v_pk_fma_f32 v[84:85], v[84:85], v[104:105], v[104:105] op_sel:[0,0,1] op_sel_hi:[1,0,1]\n\t"
    "v_pk_fma_f32 v[86:87], v[86:87], v[104:105], v[104:105] op_sel:[0,0,1] op_sel_hi:[1,0,1]\n\t"
    "v_pk_fma_f32 v[88:89], v[88:89], v[104:105], v[104:105] op_sel:[0,0,1] op_sel_hi:[1,0,1]\n\t"
    "v_pk_fma_f32 v[90:91], v[90:91], v[104:105], v[104:105] op_sel:[0,0,1] op_sel_hi:[1,0,1]\n\t"
    "v_pk_fma_f32 v[84:85], v[84:85], v[168:169], v[224:225]\n\t"
    "v_pk_fma_f32 v[86:87], v[86:87], v[170:171], v[226:227]\n\t"
    "v_pk_fma_f32 v[88:89], v[88:89], v[172:173], v[228:229]\n\t"
    "v_pk_fma_f32 v[90:91], v[90:91], v[174:175], v[230:231]\n\t"
    "v_cvt_pk_bf16_f32 v72, v84, v85\n\t"
    "v_cvt_pk_bf16_f32 v73, v86, v87\n\t"
    "v_cvt_pk_bf16_f32 v74, v88, v89\n\t"
    "v_cvt_pk_bf16_f32 v75, v90, v91\n\t"
    "ds_write_b128 v3, v[72:75] offset:4096\n\t"
    "v_lshlrev_b32 v84, 16, v80\n\t"
    "v_and_b32 v85, s39, v80\n\t"
    "v_lshlrev_b32 v86, 16, v81\n\t"
    "v_and_b32 v87, s39, v81\n\t"
    "v_lshlrev_b32 v88, 16, v82\n\t"
    "v_and_b32 v89, s39, v82\n\t"
    "v_lshlrev_b32 v90, 16, v83\n\t"
    "v_and_b32 v91, s39, v83\n\t"
    "v_pk_fma_f32 v[92:93], v[92:93], v[104:105], v[104:105] op_sel:[0,0,1] op_sel_hi:[1,0,1]\n\t"
    "v_pk_fma_f32 v[94:95], v[94:95], v[104:105], v[104:105] op_sel:[0,0,1] op_sel_hi:[1,0,1]\n\t"
    "v_pk_fma_f32 v[96:97], v[96:97], v[104:105], v[104:105] op_sel:[0,0,1] op_sel_hi:[1,0,1]\n\t"
    "v_pk_fma_f32 v[98:99], v[98:99], v[104:105], v[104:105] op_sel:[0,0,1] op_sel_hi:[1,0,1]\n\t"
    "v_pk_fma_f32 v[92:93], v[92:93], v[176:177], v[232:233]\n\t"
    "v_pk_fma_f32 v[94:95], v[94:95], v[178:179], v[234:235]\n\t"
    "v_pk_fma_f32 v[96:97], v[96:97], v[180:181], v[236:237]\n\t"
    "v_pk_fma_f32 v[98:99], v[98:99], v[182:183], v[238:239]\n\t"
    "v_cvt_pk_bf16_f32 v76, v92, v93\n\t"
    "v_cvt_pk_bf16_f32 v77, v94, v95\n\t"
    "v_cvt_pk_bf16_f32 v78, v96, v97\n\t"
    "v_cvt_pk_bf16_f32 v79, v98, v99\n\t"
    "ds_write_b128 v3, v[76:79] offset:5120\n\t"
    "v_pk_fma_f32 v[84:85], v[84:85], v[104:105], v[104:105] op_sel:[0,0,1] op_sel_hi:[1,0,1]\n\t"
    "v_pk_fma_f32 v[86:87], v[86:87], v[104:105], v[104:105] op_sel:[0,0,1] op_sel_hi:[1,0,1]\n\t"
    "v_pk_fma_f32 v[88:89], v[88:89], v[104:105], v[104:105] op_sel:[0,0,1] op_sel_hi:[1,0,1]\n\t"
    "v_pk_fma_f32 v[90:91], v[90:91], v[104:105], v[104:105] op_sel:[0,0,1] op_sel_hi:[1,0,1]\n\t"
    "v_pk_fma_f32 v[84:85], v[84:85], v[184:185], v[240:241]\n\t"
    "v_pk_fma_f32 v[86:87], v[86:87], v[186:187], v[242:243]\n\t"
    "v_pk_fma_f32 v[88:89], v[88:89], v[188:189], v[244:245]\n\t"
    "v_pk_fma_f32 v[90:91], v[90:91], v[190:191], v[246:247]\n\t"
    "v_cvt_pk_bf16_f32 v80, v84, v85\n\t"
    "v_cvt_pk_bf16_f32 v81, v86, v87\n\t"
    "v_cvt_pk_bf16_f32 v82, v88, v89\n\t"
    "v_cvt_pk_bf16_f32 v83, v90, v91\n\t"
    "ds_write_b128 v3, v[80:83] offset:6144\n\t"
    "s_waitcnt vmcnt(7)\n\t"
    "s_add_u32 m0, s42, 62720\n\t"
    "s_nop 0\n\t"
    "buffer_load_dwordx4 v1, s[16:19], s68 offen offset:768 nt lds\n\t"
    "s_waitcnt vmcnt(7)\n\t"
    "s_add_u32 m0, s42, 86016\n\t"
    "s_nop 0\n\t"
    "buffer_load_dwordx4 v1, s[16:19], s70 offen offset:0 nt lds\n\t"
    "ds_read_b128 v[56:59], v3 offset:28672\n\t"
    "ds_read_b128 v[60:63], v3 offset:29696\n\t"
    "ds_read_b128 v[64:67], v3 offset:30720\n\t"
    "ds_read_b128 v[68:71], v3 offset:31744\n\t"
    "ds_read_b128 v[72:75], v3 offset:32768\n\t"
    "ds_read_b128 v[76:79], v3 offset:33792\n\t"
    "ds_read_b128 v[80:83], v3 offset:34816\n\t"
    "s_waitcnt lgkmcnt(6)\n\t"
    "v_dot2_f32_bf16 v100, v56, s38, 0\n\t"
    "s_add_u32 m0, s42, 86912\n\t"
    "v_dot2_f32_bf16 v102, v56, v56, 0\n\t"
    "v_dot2_f32_bf16 v101, v57, s38, 0\n\t"
    "buffer_load_dwordx4 v1, s[16:19], s70 offen offset:128 nt lds\n\t"
    "v_dot2_f32_bf16 v103, v57, v57, 0\n\t"
    "v_dot2_f32_bf16 v100, v58, s38, v100\n\t"
    "v_dot2_f32_bf16 v102, v58, v58, v102\n\t"
    "v_dot2_f32_bf16 v101, v59, s38, v101\n\t"
    "v_dot2_f32_bf16 v103, v59, v59, v103\n\t"
    "s_waitcnt lgkmcnt(5)\n\t"
    "v_dot2_f32_bf16 v100, v60, s38, v100\n\t"
    "v_dot2_f32_bf16 v102, v60, v60, v102\n\t"
    "v_dot2_f32_bf16 v101, v61, s38, v101\n\t"
    "v_dot2_f32_bf16 v103, v61, v61, v103\n\t"
    "v_dot2_f32_bf16 v100, v62, s38, v100\n\t"
    "v_dot2_f32_bf16 v102, v62, v62, v102\n\t"
    "v_dot2_f32_bf16 v101, v63, s38, v101\n\t"
    "v_dot2_f32_bf16 v103, v63, v63, v103\n\t"
    "s_waitcnt lgkmcnt(4)\n\t"
    "v_dot2_f32_bf16 v100, v64, s38, v100\n\t"
    "v_dot2_f32_bf16 v102, v64, v64, v102\n\t"
    "v_dot2_f32_bf16 v101, v65, s38, v101\n\t"
    "v_dot2_f32_bf16 v103, v65, v65, v103\n\t"
    "v_dot2_f32_bf16 v100, v66, s38, v100\n\t"
    "v_dot2_f32_bf16 v102, v66, v66, v102\n\t"
    "v_dot2_f32_bf16 v101, v67, s38, v101\n\t"
    "v_dot2_f32_bf16 v103, v67, v67, v103\n\t"
    "s_waitcnt lgkmcnt(3)\n\t"
    "v_dot2_f32_bf16 v100, v68, s38, v100\n\t"
    "v_dot2_f32_bf16 v102, v68, v68, v102\n\t"
    "v_dot2_f32_bf16 v101, v69, s38, v101\n\t"
    "v_dot2_f32_bf16 v103, v69, v69, v103\n\t"
    "v_dot2_f32_bf16 v100, v70, s38, v100\n\t"
    "v_dot2_f32_bf16 v102, v70, v70, v102\n\t"
    "v_dot2_f32_bf16 v101, v71, s38, v101\n\t"
    "v_dot2_f32_bf16 v103, v71, v71, v103\n\t"
    "s_waitcnt lgkmcnt(2)\n\t"
    "v_dot2_f32_bf16 v100, v72, s38, v100\n\t"
    "v_dot2_f32_bf16 v102, v72, v72, v102\n\t"
    "v_dot2_f32_bf16 v101, v73, s38, v101\n\t"
    "v_dot2_f32_bf16 v103, v73, v73, v103\n\t"
    "v_dot2_f32_bf16 v100, v74, s38, v100\n\t"
    "v_dot2_f32_bf16 v102, v74, v74, v102\n\t"
    "v_dot2_f32_bf16 v101, v75, s38, v101\n\t"
    "v_dot2_f32_bf16 v103, v75, v75, v103\n\t"
    "s_waitcnt lgkmcnt(1)\n\t"
    "v_dot2_f32_bf16 v100, v76, s38, v100\n\t"
    "s_add_u32 m0, s42, 87808\n\t"
    "v_dot2_f32_bf16 v102, v76, v76, v102\n\t"
    "v_dot2_f32_bf16 v101, v77, s38, v101\n\t"
    "buffer_load_dwordx4 v1, s[16:19], s70 offen offset:256 nt lds\n\t"
    "v_dot2_f32_bf16 v103, v77, v77, v103\n\t"
    "v_dot2_f32_bf16 v100, v78, s38, v100\n\t"
    "v_dot2_f32_bf16 v102, v78, v78, v102\n\t"
    "v_dot2_f32_bf16 v101, v79, s38, v101\n\t"
    "v_dot2_f32_bf16 v103, v79, v79, v103\n\t"
    "s_waitcnt lgkmcnt(0)\n\t"
    "v_dot2_f32_bf16 v100, v80, s38, v100\n\t"
    "v_dot2_f32_bf16 v102, v80, v80, v102\n\t"
    "v_dot2_f32_bf16 v101, v81, s38, v101\n\t"
    "v_dot2_f32_bf16 v103, v81, v81, v103\n\t"
    "v_dot2_f32_bf16 v100, v82, s38, v100\n\t"
    "v_dot2_f32_bf16 v102, v82, v82, v102\n\t"
    "v_dot2_f32_bf16 v101, v83, s38, v101\n\t"
    "v_dot2_f32_bf16 v103, v83, v83, v103\n\t"
    "v_lshlrev_b32 v84, 16, v56\n\t"
    "v_and_b32 v85, s39, v56\n\t"
    "v_lshlrev_b32 v86, 16, v57\n\t"
    "v_add_f32 v100, v100, v101\n\t"
    "v_add_f32 v102, v102, v103\n\t"
    "v_and_b32 v87, s39, v57\n\t"
    "v_add_f32_dpp v100, v100, v100 quad_perm:[1,0,3,2] row_mask:0xf bank_mask:0xf\n\t"
    "v_add_f32_dpp v102, v102, v102 quad_perm:[1,0,3,2] row_mask:0xf bank_mask:0xf\n\t"
    "v_lshlrev_b32 v88, 16, v58\n\t"
    "v_add_f32_dpp v100, v100, v100 quad_perm:[2,3,0,1] row_mask:0xf bank_mask:0xf\n\t"
    "v_add_f32_dpp v102, v102, v102 quad_perm:[2,3,0,1] row_mask:0xf bank_mask:0xf\n\t"
    "v_and_b32 v89, s39, v58\n\t"
    "v_add_f32_dpp v100, v100, v100 row_half_mirror row_mask:0xf bank_mask:0xf\n\t"
    "v_add_f32_dpp v102, v102, v102 row_half_mirror row_mask:0xf bank_mask:0xf\n\t"
    "v_mul_f32 v101, s40, v100\n\t"
    "v_mul_f32 v103, s40, v102\n\t"
    "v_lshlrev_b32 v90, 16, v59\n\t"
    "v_fma_f32 v103, -v101, v101, v103\n\t"
    "v_and_b32 v91, s39, v59\n\t"
    "v_max_f32 v103, 0, v103\n\t"
    "v_lshlrev_b32 v92, 16, v60\n\t"
    "v_add_f32 v103, s41, v103\n\t"
    "v_and_b32 v93, s39, v60\n\t"
    "v_rsq_f32 v104, v103\n\t"
    "v_lshlrev_b32 v94, 16, v61\n\t"
    "s_add_u32 m0, s42, 88704\n\t"
    "v_and_b32 v95, s39, v61\n\t"
    "v_lshlrev_b32 v96, 16, v62\n\t"
    "buffer_load_dwordx4 v1, s[16:19], s70 offen offset:384 nt lds\n\t"
    "v_mul_f32_e64 v105, -v101, v104\n\t"
    "v_and_b32 v97, s39, v62\n\t"
    "v_lshlrev_b32 v98, 16, v63\n\t"
    "v_and_b32 v99, s39, v63\n\t"
    "v_pk_fma_f32 v[84:85], v[84:85], v[104:105], v[104:105] op_sel:[0,0,1] op_sel_hi:[1,0,1]\n\t"
    "v_pk_fma_f32 v[86:87], v[86:87], v[104:105], v[104:105] op_sel:[0,0,1] op_sel_hi:[1,0,1]\n\t"
    "v_pk_fma_f32 v[88:89], v[88:89], v[104:105], v[104:105] op_sel:[0,0,1] op_sel_hi:[1,0,1]\n\t"
    "v_pk_fma_f32 v[90:91], v[90:91], v[104:105], v[104:105] op_sel:[0,0,1] op_sel_hi:[1,0,1]\n\t"
    "v_pk_fma_f32 v[84:85], v[84:85], v[136:137], v[192:193]\n\t"
    "v_pk_fma_f32 v[86:87], v[86:87], v[138:139], v[194:195]\n\t"
    "v_pk_fma_f32 v[88:89], v[88:89], v[140:141], v[196:197]\n\t"
    "v_pk_fma_f32 v[90:91], v[90:91], v[142:143], v[198:199]\n\t"
    "v_cvt_pk_bf16_f32 v56, v84, v85\n\t"
    "v_cvt_pk_bf16_f32 v57, v86, v87\n\t"
    "v_cvt_pk_bf16_f32 v58, v88, v89\n\t"
    "v_cvt_pk_bf16_f32 v59, v90, v91\n\t"
    "ds_write_b128 v3, v[56:59] offset:28672\n\t"
    "v_lshlrev_b32 v84, 16, v64\n\t"
    "v_and_b32 v85, s39, v64\n\t"
    "v_lshlrev_b32 v86, 16, v65\n\t"
    "v_and_b32 v87, s39, v65\n\t"
    "v_lshlrev_b32 v88, 16, v66\n\t"
    "v_and_b32 v89, s39, v66\n\t"
    "v_lshlrev_b32 v90, 16, v67\n\t"
    "v_and_b32 v91, s39, v67\n\t"
    "v_pk_fma_f32 v[92:93], v[92:93], v[104:105], v[104:105] op_sel:[0,0,1] op_sel_hi:[1,0,1]\n\t"
    "v_pk_fma_f32 v[94:95], v[94:95], v[104:105], v[104:105] op_sel:[0,0,1] op_sel_hi:[1,0,1]\n\t"
    "v_pk_fma_f32 v[96:97], v[96:97], v[104:105], v[104:105] op_sel:[0,0,1] op_sel_hi:[1,0,1]\n\t"
    "v_pk_fma_f32 v[98:99], v[98:99], v[104:105], v[104:105] op_sel:[0,0,1] op_sel_hi:[1,0,1]\n\t"
    "v_pk_fma_f32 v[92:93], v[92:93], v[144:145], v[200:201]\n\t"
    "v_pk_fma_f32 v[94:95], v[94:95], v[146:147], v[202:203]\n\t"
    "v_pk_fma_f32 v[96:97], v[96:97], v[148:149], v[204:205]\n\t"
    "v_pk_fma_f32 v[98:99], v[98:99], v[150:151], v[206:207]\n\t"
    "v_cvt_pk_bf16_f32 v60, v92, v93\n\t"
    "v_cvt_pk_bf16_f32 v61, v94, v95\n\t"
    "v_cvt_pk_bf16_f32 v62, v96, v97\n\t"
    "v_cvt_pk_bf16_f32 v63, v98, v99\n\t"
    "ds_write_b128 v3, v[60:63] offset:29696\n\t"
    "s_add_u32 m0, s42, 89600\n\t"
    "v_lshlrev_b32 v92, 16, v68\n\t"
    "v_and_b32 v93, s39, v68\n\t"
    "buffer_load_dwordx4 v1, s[16:19], s70 offen offset:512 nt lds\n\t"
    "v_lshlrev_b32 v94, 16, v69\n\t"
    "v_and_b32 v95, s39, v69\n\t"
    "v_lshlrev_b32 v96, 16, v70\n\t"
    "v_and_b32 v97, s39, v70\n\t"
    "v_lshlrev_b32 v98, 16, v71\n\t"
    "v_and_b32 v99, s39, v71\n\t"
    "v_pk_fma_f32 v[84:85], v[84:85], v[104:105], v[104:105] op_sel:[0,0,1] op_sel_hi:[1,0,1]\n\t"
    "v_pk_fma_f32 v[86:87], v[86:87], v[104:105], v[104:105] op_sel:[0,0,1] op_sel_hi:[1,0,1]\n\t"
    "v_pk_fma_f32 v[88:89], v[88:89], v[104:105], v[104:105] op_sel:[0,0,1] op_sel_hi:[1,0,1]\n\t"
    "v_pk_fma_f32 v[90:91], v[90:91], v[104:105], v[104:105] op_sel:[0,0,1] op_sel_hi:[1,0,1]\n\t"
    "v_pk_fma_f32 v[84:85], v[84:85], v[152:153], v[208:209]\n\t"
    "v_pk_fma_f32 v[86:87], v[86:87], v[154:155], v[210:211]\n\t"
    "v_pk_fma_f32 v[88:89], v[88:89], v[156:157], v[212:213]\n\t"
    "v_pk_fma_f32 v[90:91], v[90:91], v[158:159], v[214:215]\n\t"
    "v_cvt_pk_bf16_f32 v64, v84, v85\n\t"
    "v_cvt_pk_bf16_f32 v65, v86, v87\n\t"
    "v_cvt_pk_bf16_f32 v66, v88, v89\n\t"
    "v_cvt_pk_bf16_f32 v67, v90, v91\n\t"
    "ds_write_b128 v3, v[64:67] offset:30720\n\t"
    "v_lshlrev_b32 v84, 16, v72\n\t"
    "v_and_b32 v85, s39, v72\n\t"
    "v_lshlrev_b32 v86, 16, v73\n\t"
    "v_and_b32 v87, s39, v73\n\t"
    "v_lshlrev_b32 v88, 16, v74\n\t"
    "v_and_b32 v89, s39, v74\n\t"
    "v_lshlrev_b32 v90, 16, v75\n\t"
    "v_and_b32 v91, s39, v75\n\t"
    "v_pk_fma_f32 v[92:93], v[92:93], v[104:105], v[104:105] op_sel:[0,0,1] op_sel_hi:[1,0,1]\n\t"
    "v_pk_fma_f32 v[94:95], v[94:95], v[104:105], v[104:105] op_sel:[0,0,1] op_sel_hi:[1,0,1]\n\t"
    "v_pk_fma_f32 v[96:97], v[96:97], v[104:105], v[104:105] op_sel:[0,0,1] op_sel_hi:[1,0,1]\n\t"
    "v_pk_fma_f32 v[98:99], v[98:99], v[104:105], v[104:105] op_sel:[0,0,1] op_sel_hi:[1,0,1]\n\t"
    "v_pk_fma_f32 v[92:93], v[92:93], v[160:161], v[216:217]\n\t"
    "v_pk_fma_f32 v[94:95], v[94:95], v[162:163], v[218:219]\n\t"
    "v_pk_fma_f32 v[96:97], v[96:97], v[164:165], v[220:221]\n\t"
    "v_pk_fma_f32 v[98:99], v[98:99], v[166:167], v[222:223]\n\t"
    "v_cvt_pk_bf16_f32 v68, v92, v93\n\t"
    "v_cvt_pk_bf16_f32 v69, v94, v95\n\t"
    "v_cvt_pk_bf16_f32 v70, v96, v97\n\t"
    "s_add_u32 m0, s42, 90496\n\t"
    "v_cvt_pk_bf16_f32 v71, v98, v99\n\t"
    "ds_write_b128 v3, v[68:71] offset:31744\n\t"
    "buffer_load_dwordx4 v1, s[16:19], s70 offen offset:640 nt lds\n\t"
    "v_lshlrev_b32 v92, 16, v76\n\t"
    "v_and_b32 v93, s39, v76\n\t"
    "v_lshlrev_b32 v94, 16, v77\n\t"
    "v_and_b32 v95, s39, v77\n\t"
    "v_lshlrev_b32 v96, 16, v78\n\t"
    "v_and_b32 v97, s39, v78\n\t"
    "v_lshlrev_b32 v98, 16, v79\n\t"
    "v_and_b32 v99, s39, v79\n\t"
    "v_pk_fma_f32 v[84:85], v[84:85], v[104:105], v[104:105] op_sel:[0,0,1] op_sel_hi:[1,0,1]\n\t"
    "v_pk_fma_f32 v[86:87], v[86:87], v[104:105], v[104:105] op_sel:[0,0,1] op_sel_hi:[1,0,1]\n\t"
    "v_pk_fma_f32 v[88:89], v[88:89], v[104:105], v[104:105] op_sel:[0,0,1] op_sel_hi:[1,0,1]\n\t"
    "v_pk_fma_f32 v[90:91], v[90:91], v[104:105], v[104:105] op_sel:[0,0,1] op_sel_hi:[1,0,1]\n\t"
    "v_pk_fma_f32 v[84:85], v[84:85], v[168:169], v[224:225]\n\t"
    "v_pk_fma_f32 v[86:87], v[86:87], v[170:171], v[226:227]\n\t"
    "v_pk_fma_f32 v[88:89], v[88:89], v[172:173], v[228:229]\n\t"
    "v_pk_fma_f32 v[90:91], v[90:91], v[174:175], v[230:231]\n\t"
    "v_cvt_pk_bf16_f32 v72, v84, v85\n\t"
    "v_cvt_pk_bf16_f32 v73, v86, v87\n\t"
    "v_cvt_pk_bf16_f32 v74, v88, v89\n\t"
    "v_cvt_pk_bf16_f32 v75, v90, v91\n\t"
    "ds_write_b128 v3, v[72:75] offset:32768\n\t"
    "v_lshlrev_b32 v84, 16, v80\n\t"
    "v_and_b32 v85, s39, v80\n\t"
    "v_lshlrev_b32 v86, 16, v81\n\t"
    "v_and_b32 v87, s39, v81\n\t"
    "v_lshlrev_b32 v88, 16, v82\n\t"
    "v_and_b32 v89, s39, v82\n\t"
    "v_lshlrev_b32 v90, 16, v83\n\t"
    "v_and_b32 v91, s39, v83\n\t"
    "v_pk_fma_f32 v[92:93], v[92:93], v[104:105], v[104:105] op_sel:[0,0,1] op_sel_hi:[1,0,1]\n\t"
    "v_pk_fma_f32 v[94:95], v[94:95], v[104:105], v[104:105] op_sel:[0,0,1] op_sel_hi:[1,0,1]\n\t"
    "v_pk_fma_f32 v[96:97], v[96:97], v[104:105], v[104:105] op_sel:[0,0,1] op_sel_hi:[1,0,1]\n\t"
    "v_pk_fma_f32 v[98:99], v[98:99], v[104:105], v[104:105] op_sel:[0,0,1] op_sel_hi:[1,0,1]\n\t"
    "v_pk_fma_f32 v[92:93], v[92:93], v[176:177], v[232:233]\n\t"
    "v_pk_fma_f32 v[94:95], v[94:95], v[178:179], v[234:235]\n\t"
    "v_pk_fma_f32 v[96:97], v[96:97], v[180:181], v[236:237]\n\t"
    "v_pk_fma_f32 v[98:99], v[98:99], v[182:183], v[238:239]\n\t"
    "v_cvt_pk_bf16_f32 v76, v92, v93\n\t"
    "v_cvt_pk_bf16_f32 v77, v94, v95\n\t"
    "v_cvt_pk_bf16_f32 v78, v96, v97\n\t"
    "v_cvt_pk_bf16_f32 v79, v98, v99\n\t"
    "ds_write_b128 v3, v[76:79] offset:33792\n\t"
    "v_pk_fma_f32 v[84:85], v[84:85], v[104:105], v[104:105] op_sel:[0,0,1] op_sel_hi:[1,0,1]\n\t"
    "v_pk_fma_f32 v[86:87], v[86:87], v[104:105], v[104:105] op_sel:[0,0,1] op_sel_hi:[1,0,1]\n\t"
    "v_pk_fma_f32 v[88:89], v[88:89], v[104:105], v[104:105] op_sel:[0,0,1] op_sel_hi:[1,0,1]\n\t"
    "v_pk_fma_f32 v[90:91], v[90:91], v[104:105], v[104:105] op_sel:[0,0,1] op_sel_hi:[1,0,1]\n\t"
    "v_pk_fma_f32 v[84:85], v[84:85], v[184:185], v[240:241]\n\t"
    "v_pk_fma_f32 v[86:87], v[86:87], v[186:187], v[242:243]\n\t"
    "v_pk_fma_f32 v[88:89], v[88:89], v[188:189], v[244:245]\n\t"
    "v_pk_fma_f32 v[90:91], v[90:91], v[190:191], v[246:247]\n\t"
    "v_cvt_pk_bf16_f32 v80, v84, v85\n\t"
    "v_cvt_pk_bf16_f32 v81, v86, v87\n\t"
    "v_cvt_pk_bf16_f32 v82, v88, v89\n\t"
    "v_cvt_pk_bf16_f32 v83, v90, v91\n\t"
    "ds_write_b128 v3, v[80:83] offset:34816\n\t"
    "s_waitcnt vmcnt(6)\n\t"
    "s_add_u32 m0, s42, 91392\n\t"
    "s_nop 0\n\t"
    "buffer_load_dwordx4 v1, s[16:19], s70 offen offset:768 nt lds\n\t"
    "ds_read_b128 v[56:59], v4 offset:0\n\t"
    "ds_read_b128 v[60:63], v4 offset:1024\n\t"
    "ds_read_b128 v[64:67], v4 offset:2048\n\t"
    "ds_read_b128 v[68:71], v4 offset:3072\n\t"
    "ds_read_b128 v[72:75], v4 offset:4096\n\t"
    "ds_read_b128 v[76:79], v4 offset:5120\n\t"
    "ds_read_b128 v[80:83], v4 offset:6144\n\t"
    "s_waitcnt lgkmcnt(6)\n\t"
    "v_dot2_f32_bf16 v100, v56, s38, 0\n\t"
    "v_dot2_f32_bf16 v102, v56, v56, 0\n\t"
    "v_dot2_f32_bf16 v101, v57, s38, 0\n\t"
    "v_dot2_f32_bf16 v103, v57, v57, 0\n\t"
    "v_dot2_f32_bf16 v100, v58, s38, v100\n\t"
    "v_dot2_f32_bf16 v102, v58, v58, v102\n\t"
    "v_dot2_f32_bf16 v101, v59, s38, v101\n\t"
    "v_dot2_f32_bf16 v103, v59, v59, v103\n\t"
    "s_waitcnt lgkmcnt(5)\n\t"
    "v_dot2_f32_bf16 v100, v60, s38, v100\n\t"
    "v_dot2_f32_bf16 v102, v60, v60, v102\n\t"
    "v_dot2_f32_bf16 v101, v61, s38, v101\n\t"
    "v_dot2_f32_bf16 v103, v61, v61, v103\n\t"
    "v_dot2_f32_bf16 v100, v62, s38, v100\n\t"
    "v_dot2_f32_bf16 v102, v62, v62, v102\n\t"
    "v_dot2_f32_bf16 v101, v63, s38, v101\n\t"
    "v_dot2_f32_bf16 v103, v63, v63, v103\n\t"
    "s_waitcnt lgkmcnt(4)\n\t"
    "v_dot2_f32_bf16 v100, v64, s38, v100\n\t"
    "v_dot2_f32_bf16 v102, v64, v64, v102\n\t"
    "v_dot2_f32_bf16 v101, v65, s38, v101\n\t"
    "v_dot2_f32_bf16 v103, v65, v65, v103\n\t"
    "v_dot2_f32_bf16 v100, v66, s38, v100\n\t"
    "v_dot2_f32_bf16 v102, v66, v66, v102\n\t"
    "v_dot2_f32_bf16 v101, v67, s38, v101\n\t"
    "v_dot2_f32_bf16 v103, v67, v67, v103\n\t"
    "s_waitcnt lgkmcnt(3)\n\t"
    "v_dot2_f32_bf16 v100, v68, s38, v100\n\t"
    "v_dot2_f32_bf16 v102, v68, v68, v102\n\t"
    "v_dot2_f32_bf16 v101, v69, s38, v101\n\t"
    "v_dot2_f32_bf16 v103, v69, v69, v103\n\t"
    "v_dot2_f32_bf16 v100, v70, s38, v100\n\t"
    "v_dot2_f32_bf16 v102, v70, v70, v102\n\t"
    "v_dot2_f32_bf16 v101, v71, s38, v101\n\t"
    "v_dot2_f32_bf16 v103, v71, v71, v103\n\t"
    "s_waitcnt lgkmcnt(2)\n\t"
    "v_dot2_f32_bf16 v100, v72, s38, v100\n\t"
    "v_dot2_f32_bf16 v102, v72, v72, v102\n\t"
    "v_dot2_f32_bf16 v101, v73, s38, v101\n\t"
    "v_dot2_f32_bf16 v103, v73, v73, v103\n\t"
    "v_dot2_f32_bf16 v100, v74, s38, v100\n\t"
    "v_dot2_f32_bf16 v102, v74, v74, v102\n\t"
    "v_dot2_f32_bf16 v101, v75, s38, v101\n\t"
    "v_dot2_f32_bf16 v103, v75, v75, v103\n\t"
    "s_waitcnt lgkmcnt(1)\n\t"
    "v_dot2_f32_bf16 v100, v76, s38, v100\n\t"
    "v_dot2_f32_bf16 v102, v76, v76, v102\n\t"
    "v_dot2_f32_bf16 v101, v77, s38, v101\n\t"
    "v_dot2_f32_bf16 v103, v77, v77, v103\n\t"
    "v_dot2_f32_bf16 v100, v78, s38, v100\n\t"
    "v_dot2_f32_bf16 v102, v78, v78, v102\n\t"
    "v_dot2_f32_bf16 v101, v79, s38, v101\n\t"
    "v_dot2_f32_bf16 v103, v79, v79, v103\n\t"
    "s_waitcnt lgkmcnt(0)\n\t"
    "v_dot2_f32_bf16 v100, v80, s38, v100\n\t"
    "v_dot2_f32_bf16 v102, v80, v80, v102\n\t"
    "v_dot2_f32_bf16 v101, v81, s38, v101\n\t"
    "v_dot2_f32_bf16 v103, v81, v81, v103\n\t"
    "v_dot2_f32_bf16 v100, v82, s38, v100\n\t"
    "v_dot2_f32_bf16 v102, v82, v82, v102\n\t"
    "v_dot2_f32_bf16 v101, v83, s38, v101\n\t"
    "v_dot2_f32_bf16 v103, v83, v83, v103\n\t"
    "v_lshlrev_b32 v84, 16, v56\n\t"
    "v_and_b32 v85, s39, v56\n\t"
    "v_lshlrev_b32 v86, 16, v57\n\t"
    "v_add_f32 v100, v100, v101\n\t"
    "v_add_f32 v102, v102, v103\n\t"
    "v_and_b32 v87, s39, v57\n\t"
    "v_add_f32_dpp v100, v100, v100 quad_perm:[1,0,3,2] row_mask:0xf bank_mask:0xf\n\t"
    "v_add_f32_dpp v102, v102, v102 quad_perm:[1,0,3,2] row_mask:0xf bank_mask:0xf\n\t"
    "v_lshlrev_b32 v88, 16, v58\n\t"
    "v_add_f32_dpp v100, v100, v100 quad_perm:[2,3,0,1] row_mask:0xf bank_mask:0xf\n\t"
    "v_add_f32_dpp v102, v102, v102 quad_perm:[2,3,0,1] row_mask:0xf bank_mask:0xf\n\t"
    "v_and_b32 v89, s39, v58\n\t"
    "v_add_f32_dpp v100, v100, v100 row_half_mirror row_mask:0xf bank_mask:0xf\n\t"
    "v_add_f32_dpp v102, v102, v102 row_half_mirror row_mask:0xf bank_mask:0xf\n\t"
    "v_mul_f32 v101, s40, v100\n\t"
    "v_mul_f32 v103, s40, v102\n\t"
    "v_lshlrev_b32 v90, 16, v59\n\t"
    "v_fma_f32 v103, -v101, v101, v103\n\t"
    "v_and_b32 v91, s39, v59\n\t"
    "v_max_f32 v103, 0, v103\n\t"
    "v_lshlrev_b32 v92, 16, v60\n\t"
    "v_add_f32 v103, s41, v103\n\t"
    "v_and_b32 v93, s39, v60\n\t"
    "v_rsq_f32 v104, v103\n\t"
    "v_lshlrev_b32 v94, 16, v61\n\t"
    "v_and_b32 v95, s39, v61\n\t"
    "v_lshlrev_b32 v96, 16, v62\n\t"
    "v_mul_f32_e64 v105, -v101, v104\n\t"
    "v_and_b32 v97, s39, v62\n\t"
    "v_lshlrev_b32 v98, 16, v63\n\t"
    "v_and_b32 v99, s39, v63\n\t"
    "v_pk_fma_f32 v[84:85], v[84:85], v[104:105], v[104:105] op_sel:[0,0,1] op_sel_hi:[1,0,1]\n\t"
    "v_pk_fma_f32 v[86:87], v[86:87], v[104:105], v[104:105] op_sel:[0,0,1] op_sel_hi:[1,0,1]\n\t"
    "v_pk_fma_f32 v[88:89], v[88:89], v[104:105], v[104:105] op_sel:[0,0,1] op_sel_hi:[1,0,1]\n\t"
    "v_pk_fma_f32 v[90:91], v[90:91], v[104:105], v[104:105] op_sel:[0,0,1] op_sel_hi:[1,0,1]\n\t"
    "v_pk_fma_f32 v[84:85], v[84:85], v[136:137], v[192:193]\n\t"
    "v_pk_fma_f32 v[86:87], v[86:87], v[138:139], v[194:195]\n\t"
    "v_pk_fma_f32 v[88:89], v[88:89], v[140:141], v[196:197]\n\t"
    "v_pk_fma_f32 v[90:91], v[90:91], v[142:143], v[198:199]\n\t"
    "v_cvt_pk_bf16_f32 v56, v84, v85\n\t"
    "v_cvt_pk_bf16_f32 v57, v86, v87\n\t"
    "v_cvt_pk_bf16_f32 v58, v88, v89\n\t"
    "v_cvt_pk_bf16_f32 v59, v90, v91\n\t"
    "ds_write_b128 v4, v[56:59] offset:0\n\t"
    "v_lshlrev_b32 v84, 16, v64\n\t"
    "v_and_b32 v85, s39, v64\n\t"
    "v_lshlrev_b32 v86, 16, v65\n\t"
    "v_and_b32 v87, s39, v65\n\t"
    "v_lshlrev_b32 v88, 16, v66\n\t"
    "v_and_b32 v89, s39, v66\n\t"
    "v_lshlrev_b32 v90, 16, v67\n\t"
    "v_and_b32 v91, s39, v67\n\t"
    "v_pk_fma_f32 v[92:93], v[92:93], v[104:105], v[104:105] op_sel:[0,0,1] op_sel_hi:[1,0,1]\n\t"
    "v_pk_fma_f32 v[94:95], v[94:95], v[104:105], v[104:105] op_sel:[0,0,1] op_sel_hi:[1,0,1]\n\t"
    "v_pk_fma_f32 v[96:97], v[96:97], v[104:105], v[104:105] op_sel:[0,0,1] op_sel_hi:[1,0,1]\n\t"
    "v_pk_fma_f32 v[98:99], v[98:99], v[104:105], v[104:105] op_sel:[0,0,1] op_sel_hi:[1,0,1]\n\t"
    "v_pk_fma_f32 v[92:93], v[92:93], v[144:145], v[200:201]\n\t"
    "v_pk_fma_f32 v[94:95], v[94:95], v[146:147], v[202:203]\n\t"
    "v_pk_fma_f32 v[96:97], v[96:97], v[148:149], v[204:205]\n\t"
    "v_pk_fma_f32 v[98:99], v[98:99], v[150:151], v[206:207]\n\t"
    "v_cvt_pk_bf16_f32 v60, v92, v93\n\t"
    "v_cvt_pk_bf16_f32 v61, v94, v95\n\t"
    "v_cvt_pk_bf16_f32 v62, v96, v97\n\t"
    "v_cvt_pk_bf16_f32 v63, v98, v99\n\t"
    "ds_write_b128 v4, v[60:63] offset:1024\n\t"
    "v_lshlrev_b32 v92, 16, v68\n\t"
    "v_and_b32 v93, s39, v68\n\t"
    "v_lshlrev_b32 v94, 16, v69\n\t"
    "v_and_b32 v95, s39, v69\n\t"
    "v_lshlrev_b32 v96, 16, v70\n\t"
    "v_and_b32 v97, s39, v70\n\t"
    "v_lshlrev_b32 v98, 16, v71\n\t"
    "v_and_b32 v99, s39, v71\n\t"
    "v_pk_fma_f32 v[84:85], v[84:85], v[104:105], v[104:105] op_sel:[0,0,1] op_sel_hi:[1,0,1]\n\t"
    "v_pk_fma_f32 v[86:87], v[86:87], v[104:105], v[104:105] op_sel:[0,0,1] op_sel_hi:[1,0,1]\n\t"
    "v_pk_fma_f32 v[88:89], v[88:89], v[104:105], v[104:105] op_sel:[0,0,1] op_sel_hi:[1,0,1]\n\t"
    "v_pk_fma_f32 v[90:91], v[90:91], v[104:105], v[104:105] op_sel:[0,0,1] op_sel_hi:[1,0,1]\n\t"
    "v_pk_fma_f32 v[84:85], v[84:85], v[152:153], v[208:209]\n\t"
    "v_pk_fma_f32 v[86:87], v[86:87], v[154:155], v[210:211]\n\t"
    "v_pk_fma_f32 v[88:89], v[88:89], v[156:157], v[212:213]\n\t"
    "v_pk_fma_f32 v[90:91], v[90:91], v[158:159], v[214:215]\n\t"
    "v_cvt_pk_bf16_f32 v64, v84, v85\n\t"
    "v_cvt_pk_bf16_f32 v65, v86, v87\n\t"
    "v_cvt_pk_bf16_f32 v66, v88, v89\n\t"
    "v_cvt_pk_bf16_f32 v67, v90, v91\n\t"
    "ds_write_b128 v4, v[64:67] offset:2048\n\t"
    "v_lshlrev_b32 v84, 16, v72\n\t"
    "v_and_b32 v85, s39, v72\n\t"
    "v_lshlrev_b32 v86, 16, v73\n\t"
    "v_and_b32 v87, s39, v73\n\t"
    "v_lshlrev_b32 v88, 16, v74\n\t"
    "v_and_b32 v89, s39, v74\n\t"
    "v_lshlrev_b32 v90, 16, v75\n\t"
    "v_and_b32 v91, s39, v75\n\t"
    "v_pk_fma_f32 v[92:93], v[92:93], v[104:105], v[104:105] op_sel:[0,0,1] op_sel_hi:[1,0,1]\n\t"
    "v_pk_fma_f32 v[94:95], v[94:95], v[104:105], v[104:105] op_sel:[0,0,1] op_sel_hi:[1,0,1]\n\t"
    "v_pk_fma_f32 v[96:97], v[96:97], v[104:105], v[104:105] op_sel:[0,0,1] op_sel_hi:[1,0,1]\n\t"
    "v_pk_fma_f32 v[98:99], v[98:99], v[104:105], v[104:105] op_sel:[0,0,1] op_sel_hi:[1,0,1]\n\t"
    "v_pk_fma_f32 v[92:93], v[92:93], v[160:161], v[216:217]\n\t"
    "v_pk_fma_f32 v[94:95], v[94:95], v[162:163], v[218:219]\n\t"
    "v_pk_fma_f32 v[96:97], v[96:97], v[164:165], v[220:221]\n\t"
    "v_pk_fma_f32 v[98:99], v[98:99], v[166:167], v[222:223]\n\t"
    "v_cvt_pk_bf16_f32 v68, v92, v93\n\t"
    "v_cvt_pk_bf16_f32 v69, v94, v95\n\t"
    "v_cvt_pk_bf16_f32 v70, v96, v97\n\t"
    "v_cvt_pk_bf16_f32 v71, v98, v99\n\t"
    "ds_write_b128 v4, v[68:71] offset:3072\n\t"
    "v_lshlrev_b32 v92, 16, v76\n\t"
    "v_and_b32 v93, s39, v76\n\t"
    "v_lshlrev_b32 v94, 16, v77\n\t"
    "v_and_b32 v95, s39, v77\n\t"
    "v_lshlrev_b32 v96, 16, v78\n\t"
    "v_and_b32 v97, s39, v78\n\t"
    "v_lshlrev_b32 v98, 16, v79\n\t"
    "v_and_b32 v99, s39, v79\n\t"
    "v_pk_fma_f32 v[84:85], v[84:85], v[104:105], v[104:105] op_sel:[0,0,1] op_sel_hi:[1,0,1]\n\t"
    "v_pk_fma_f32 v[86:87], v[86:87], v[104:105], v[104:105] op_sel:[0,0,1] op_sel_hi:[1,0,1]\n\t"
    "v_pk_fma_f32 v[88:89], v[88:89], v[104:105], v[104:105] op_sel:[0,0,1] op_sel_hi:[1,0,1]\n\t"
    "v_pk_fma_f32 v[90:91], v[90:91], v[104:105], v[104:105] op_sel:[0,0,1] op_sel_hi:[1,0,1]\n\t"
    "v_pk_fma_f32 v[84:85], v[84:85], v[168:169], v[224:225]\n\t"
    "v_pk_fma_f32 v[86:87], v[86:87], v[170:171], v[226:227]\n\t"
    "v_pk_fma_f32 v[88:89], v[88:89], v[172:173], v[228:229]\n\t"
    "v_pk_fma_f32 v[90:91], v[90:91], v[174:175], v[230:231]\n\t"
    "v_cvt_pk_bf16_f32 v72, v84, v85\n\t"
    "v_cvt_pk_bf16_f32 v73, v86, v87\n\t"
    "v_cvt_pk_bf16_f32 v74, v88, v89\n\t"
    "v_cvt_pk_bf16_f32 v75, v90, v91\n\t"
    "ds_write_b128 v4, v[72:75] offset:4096\n\t"
    "v_lshlrev_b32 v84, 16, v80\n\t"
    "v_and_b32 v85, s39, v80\n\t"
    "v_lshlrev_b32 v86, 16, v81\n\t"
    "v_and_b32 v87, s39, v81\n\t"
    "v_lshlrev_b32 v88, 16, v82\n\t"
    "v_and_b32 v89, s39, v82\n\t"
    "v_lshlrev_b32 v90, 16, v83\n\t"
    "v_and_b32 v91, s39, v83\n\t"
    "v_pk_fma_f32 v[92:93], v[92:93], v[104:105], v[104:105] op_sel:[0,0,1] op_sel_hi:[1,0,1]\n\t"
    "v_pk_fma_f32 v[94:95], v[94:95], v[104:105], v[104:105] op_sel:[0,0,1] op_sel_hi:[1,0,1]\n\t"
    "v_pk_fma_f32 v[96:97], v[96:97], v[104:105], v[104:105] op_sel:[0,0,1] op_sel_hi:[1,0,1]\n\t"
    "v_pk_fma_f32 v[98:99], v[98:99], v[104:105], v[104:105] op_sel:[0,0,1] op_sel_hi:[1,0,1]\n\t"
    "v_pk_fma_f32 v[92:93], v[92:93], v[176:177], v[232:233]\n\t"
    "v_pk_fma_f32 v[94:95], v[94:95], v[178:179], v[234:235]\n\t"
    "v_pk_fma_f32 v[96:97], v[96:97], v[180:181], v[236:237]\n\t"
    "v_pk_fma_f32 v[98:99], v[98:99], v[182:183], v[238:239]\n\t"
    "v_cvt_pk_bf16_f32 v76, v92, v93\n\t"
    "v_cvt_pk_bf16_f32 v77, v94, v95\n\t"
    "v_cvt_pk_bf16_f32 v78, v96, v97\n\t"
    "v_cvt_pk_bf16_f32 v79, v98, v99\n\t"
    "ds_write_b128 v4, v[76:79] offset:5120\n\t"
    "v_pk_fma_f32 v[84:85], v[84:85], v[104:105], v[104:105] op_sel:[0,0,1] op_sel_hi:[1,0,1]\n\t"
    "v_pk_fma_f32 v[86:87], v[86:87], v[104:105], v[104:105] op_sel:[0,0,1] op_sel_hi:[1,0,1]\n\t"
    "v_pk_fma_f32 v[88:89], v[88:89], v[104:105], v[104:105] op_sel:[0,0,1] op_sel_hi:[1,0,1]\n\t"
    "v_pk_fma_f32 v[90:91], v[90:91], v[104:105], v[104:105] op_sel:[0,0,1] op_sel_hi:[1,0,1]\n\t"
    "v_pk_fma_f32 v[84:85], v[84:85], v[184:185], v[240:241]\n\t"
    "v_pk_fma_f32 v[86:87], v[86:87], v[186:187], v[242:243]\n\t"
    "v_pk_fma_f32 v[88:89], v[88:89], v[188:189], v[244:245]\n\t"
    "v_pk_fma_f32 v[90:91], v[90:91], v[190:191], v[246:247]\n\t"
    "v_cvt_pk_bf16_f32 v80, v84, v85\n\t"
    "v_cvt_pk_bf16_f32 v81, v86, v87\n\t"
    "v_cvt_pk_bf16_f32 v82, v88, v89\n\t"
    "v_cvt_pk_bf16_f32 v83, v90, v91\n\t"
    "ds_write_b128 v4, v[80:83] offset:6144\n\t"
    "s_waitcnt vmcnt(6)\n\t"
    "ds_read_b128 v[56:59], v4 offset:28672\n\t"
    "s_waitcnt vmcnt(5)\n\t"
    "ds_read_b128 v[60:63], v4 offset:29696\n\t"
    "s_waitcnt lgkmcnt(1)\n\t"
    "v_dot2_f32_bf16 v100, v56, s38, 0\n\t"
    "v_dot2_f32_bf16 v102, v56, v56, 0\n\t"
    "v_dot2_f32_bf16 v101, v57, s38, 0\n\t"
    "v_dot2_f32_bf16 v103, v57, v57, 0\n\t"
    "v_dot2_f32_bf16 v100, v58, s38, v100\n\t"
    "v_dot2_f32_bf16 v102, v58, v58, v102\n\t"
    "v_dot2_f32_bf16 v101, v59, s38, v101\n\t"
    "v_dot2_f32_bf16 v103, v59, v59, v103\n\t"
    "v_lshlrev_b32 v24, 16, v56\n\t"
    "v_and_b32 v25, s39, v56\n\t"
    "v_lshlrev_b32 v26, 16, v57\n\t"
    "v_and_b32 v27, s39, v57\n\t"
    "v_lshlrev_b32 v28, 16, v58\n\t"
    "v_and_b32 v29, s39, v58\n\t"
    "v_lshlrev_b32 v30, 16, v59\n\t"
    "v_and_b32 v31, s39, v59\n\t"
    "s_waitcnt vmcnt(4)\n\t"
    "ds_read_b128 v[64:67], v4 offset:30720\n\t"
    "s_waitcnt lgkmcnt(1)\n\t"
    "v_dot2_f32_bf16 v100, v60, s38, v100\n\t"
    "v_dot2_f32_bf16 v102, v60, v60, v102\n\t"
    "v_dot2_f32_bf16 v101, v61, s38, v101\n\t"
    "v_dot2_f32_bf16 v103, v61, v61, v103\n\t"
    "v_dot2_f32_bf16 v100, v62, s38, v100\n\t"
    "v_dot2_f32_bf16 v102, v62, v62, v102\n\t"
    "v_dot2_f32_bf16 v101, v63, s38, v101\n\t"
    "v_dot2_f32_bf16 v103, v63, v63, v103\n\t"
    "v_lshlrev_b32 v32, 16, v60\n\t"
    "v_and_b32 v33, s39, v60\n\t"
    "v_lshlrev_b32 v34, 16, v61\n\t"
    "v_and_b32 v35, s39, v61\n\t"
    "v_lshlrev_b32 v36, 16, v62\n\t"
    "v_and_b32 v37, s39, v62\n\t"
    "v_lshlrev_b32 v38, 16, v63\n\t"
    "v_and_b32 v39, s39, v63\n\t"
    "s_waitcnt vmcnt(3)\n\t"
    "ds_read_b128 v[68:71], v4 offset:31744\n\t"
    "s_add_u32 m0, s43, 0\n\t"
    "s_nop 0\n\t"
    "buffer_load_dwordx4 v2, s[20:23], s64 offen  lds\n\t"
    "s_add_u32 m0, s43, 1024\n\t"
    "s_nop 0\n\t"
    "buffer_load_dwordx4 v2, s[20:23], s65 offen  lds\n\t"
    "s_waitcnt lgkmcnt(1)\n\t"
    "v_dot2_f32_bf16 v100, v64, s38, v100\n\t"
    "v_dot2_f32_bf16 v102, v64, v64, v102\n\t"
    "v_dot2_f32_bf16 v101, v65, s38, v101\n\t"
    "v_dot2_f32_bf16 v103, v65, v65, v103\n\t"
    "v_dot2_f32_bf16 v100, v66, s38, v100\n\t"
    "v_dot2_f32_bf16 v102, v66, v66, v102\n\t"
    "v_dot2_f32_bf16 v101, v67, s38, v101\n\t"
    "v_dot2_f32_bf16 v103, v67, v67, v103\n\t"
    "v_lshlrev_b32 v40, 16, v64\n\t"
    "v_and_b32 v41, s39, v64\n\t"
    "v_lshlrev_b32 v42, 16, v65\n\t"
    "v_and_b32 v43, s39, v65\n\t"
    "v_lshlrev_b32 v44, 16, v66\n\t"
    "v_and_b32 v45, s39, v66\n\t"
    "v_lshlrev_b32 v46, 16, v67\n\t"
    "v_and_b32 v47, s39, v67\n\t"
    "s_waitcnt vmcnt(4)\n\t"
    "ds_read_b128 v[72:75], v4 offset:32768\n\t"
    "s_add_u32 m0, s43, 2048\n\t"
    "s_nop 0\n\t"
    "buffer_load_dwordx4 v2, s[20:23], s66 offen  lds\n\t"
    "s_add_u32 m0, s43, 3072\n\t"
    "s_nop 0\n\t"
    "buffer_load_dwordx4 v2, s[20:23], s67 offen  lds\n\t"
    "s_waitcnt lgkmcnt(1)\n\t"
    "v_dot2_f32_bf16 v100, v68, s38, v100\n\t"
    "v_dot2_f32_bf16 v102, v68, v68, v102\n\t"
    "v_dot2_f32_bf16 v101, v69, s38, v101\n\t"
    "v_dot2_f32_bf16 v103, v69, v69, v103\n\t"
    "v_dot2_f32_bf16 v100, v70, s38, v100\n\t"
    "v_dot2_f32_bf16 v102, v70, v70, v102\n\t"
    "v_dot2_f32_bf16 v101, v71, s38, v101\n\t"
    "v_dot2_f32_bf16 v103, v71, v71, v103\n\t"
    "v_lshlrev_b32 v48, 16, v68\n\t"
    "v_and_b32 v49, s39, v68\n\t"
    "v_lshlrev_b32 v50, 16, v69\n\t"
    "v_and_b32 v51, s39, v69\n\t"
    "v_lshlrev_b32 v52, 16, v70\n\t"
    "v_and_b32 v53, s39, v70\n\t"
    "v_lshlrev_b32 v54, 16, v71\n\t"
    "v_and_b32 v55, s39, v71\n\t"
    "s_waitcnt vmcnt(5)\n\t"
    "ds_read_b128 v[76:79], v4 offset:33792\n\t"
    "s_add_u32 m0, s43, 4096\n\t"
    "s_nop 0\n\t"
    "buffer_load_dwordx4 v2, s[20:23], s68 offen  lds\n\t"
    "s_add_u32 m0, s43, 5120\n\t"
    "s_nop 0\n\t"
    "buffer_load_dwordx4 v2, s[20:23], s69 offen  lds\n\t"
    "s_waitcnt lgkmcnt(1)\n\t"
    "v_dot2_f32_bf16 v100, v72, s38, v100\n\t"
    "v_dot2_f32_bf16 v102, v72, v72, v102\n\t"
    "v_dot2_f32_bf16 v101, v73, s38, v101\n\t"
    "v_dot2_f32_bf16 v103, v73, v73, v103\n\t"
    "v_dot2_f32_bf16 v100, v74, s38, v100\n\t"
    "v_dot2_f32_bf16 v102, v74, v74, v102\n\t"
    "v_dot2_f32_bf16 v101, v75, s38, v101\n\t"
    "v_dot2_f32_bf16 v103, v75, v75, v103\n\t"
    "v_lshlrev_b32 v112, 16, v72\n\t"
    "v_and_b32 v113, s39, v72\n\t"
    "v_lshlrev_b32 v114, 16, v73\n\t"
    "v_and_b32 v115, s39, v73\n\t"
    "v_lshlrev_b32 v116, 16, v74\n\t"
    "v_and_b32 v117, s39, v74\n\t"
    "v_lshlrev_b32 v118, 16, v75\n\t"
    "v_and_b32 v119, s39, v75\n\t"
    "s_waitcnt vmcnt(6)\n\t"
    "ds_read_b128 v[80:83], v4 offset:34816\n\t"
    "buffer_load_dword a255, v255, s[20:23], 0 offen\n\t"
    "s_add_u32 m0, s43, 6144\n\t"
    "s_nop 0\n\t"
    "buffer_load_dwordx4 v2, s[20:23], s70 offen  lds\n\t"
    "s_add_u32 m0, s43, 7168\n\t"
    "s_nop 0\n\t"
    "buffer_load_dwordx4 v2, s[20:23], s71 offen  lds\n\t"
    "s_waitcnt lgkmcnt(1)\n\t"
    "v_dot2_f32_bf16 v100, v76, s38, v100\n\t"
    "v_dot2_f32_bf16 v102, v76, v76, v102\n\t"
    "v_dot2_f32_bf16 v101, v77, s38, v101\n\t"
    "v_dot2_f32_bf16 v103, v77, v77, v103\n\t"
    "v_dot2_f32_bf16 v100, v78, s38, v100\n\t"
    "v_dot2_f32_bf16 v102, v78, v78, v102\n\t"
    "v_dot2_f32_bf16 v101, v79, s38, v101\n\t"
    "v_dot2_f32_bf16 v103, v79, v79, v103\n\t"
    "v_lshlrev_b32 v120, 16, v76\n\t"
    "v_and_b32 v121, s39, v76\n\t"
    "v_lshlrev_b32 v122, 16, v77\n\t"
    "v_and_b32 v123, s39, v77\n\t"
    "v_lshlrev_b32 v124, 16, v78\n\t"
    "v_and_b32 v125, s39, v78\n\t"
    "v_lshlrev_b32 v126, 16, v79\n\t"
    "v_and_b32 v127, s39, v79\n\t"
    "s_waitcnt lgkmcnt(0)\n\t"
    "v_dot2_f32_bf16 v100, v80, s38, v100\n\t"
    "s_add_u32 m0, s43, 8192\n\t"
    "v_dot2_f32_bf16 v102, v80, v80, v102\n\t"
    "v_dot2_f32_bf16 v101, v81, s38, v101\n\t"
    "buffer_load_dwordx4 v2, s[20:23], s72 offen  lds\n\t"
    "v_dot2_f32_bf16 v103, v81, v81, v103\n\t"
    "v_dot2_f32_bf16 v100, v82, s38, v100\n\t"
    "v_dot2_f32_bf16 v102, v82, v82, v102\n\t"
    "v_dot2_f32_bf16 v101, v83, s38, v101\n\t"
    "v_dot2_f32_bf16 v103, v83, v83, v103\n\t"
    "v_lshlrev_b32 v84, 16, v80\n\t"
    "v_and_b32 v85, s39, v80\n\t"
    "v_lshlrev_b32 v86, 16, v81\n\t"
    "s_add_u32 m0, s43, 9216\n\t"
    "v_add_f32 v100, v100, v101\n\t"
    "v_add_f32 v102, v102, v103\n\t"
    "buffer_load_dwordx4 v2, s[20:23], s73 offen  lds\n\t"
    "v_and_b32 v87, s39, v81\n\t"
    "v_add_f32_dpp v100, v100, v100 quad_perm:[1,0,3,2] row_mask:0xf bank_mask:0xf\n\t"
    "v_add_f32_dpp v102, v102, v102 quad_perm:[1,0,3,2] row_mask:0xf bank_mask:0xf\n\t"
    "v_lshlrev_b32 v88, 16, v82\n\t"
    "v_add_f32_dpp v100, v100, v100 quad_perm:[2,3,0,1] row_mask:0xf bank_mask:0xf\n\t"
    "v_add_f32_dpp v102, v102, v102 quad_perm:[2,3,0,1] row_mask:0xf bank_mask:0xf\n\t"
    "v_and_b32 v89, s39, v82\n\t"
    "v_add_f32_dpp v100, v100, v100 row_half_mirror row_mask:0xf bank_mask:0xf\n\t"
    "v_add_f32_dpp v102, v102, v102 row_half_mirror row_mask:0xf bank_mask:0xf\n\t"
    "v_mul_f32 v101, s40, v100\n\t"
    "v_mul_f32 v103, s40, v102\n\t"
    "v_lshlrev_b32 v90, 16, v83\n\t"
    "v_fma_f32 v103, -v101, v101, v103\n\t"
    "v_and_b32 v91, s39, v83\n\t"
    "v_max_f32 v103, 0, v103\n\t"
    "v_add_f32 v103, s41, v103\n\t"
    "v_rsq_f32 v104, v103\n\t"
    "s_nop 0\n\t"
    "v_mul_f32_e64 v105, -v101, v104\n\t"
    "v_pk_fma_f32 v[24:25], v[24:25], v[104:105], v[104:105] op_sel:[0,0,1] op_sel_hi:[1,0,1]\n\t"
    "v_pk_fma_f32 v[26:27], v[26:27], v[104:105], v[104:105] op_sel:[0,0,1] op_sel_hi:[1,0,1]\n\t"
    "v_pk_fma_f32 v[28:29], v[28:29], v[104:105], v[104:105] op_sel:[0,0,1] op_sel_hi:[1,0,1]\n\t"
    "v_pk_fma_f32 v[30:31], v[30:31], v[104:105], v[104:105] op_sel:[0,0,1] op_sel_hi:[1,0,1]\n\t"
    "v_pk_fma_f32 v[24:25], v[24:25], v[136:137], v[192:193]\n\t"
    "v_pk_fma_f32 v[26:27], v[26:27], v[138:139], v[194:195]\n\t"
    "v_pk_fma_f32 v[28:29], v[28:29], v[140:141], v[196:197]\n\t"
    "v_pk_fma_f32 v[30:31], v[30:31], v[142:143], v[198:199]\n\t"
    "v_cvt_pk_bf16_f32 v56, v24, v25\n\t"
    "v_cvt_pk_bf16_f32 v57, v26, v27\n\t"
    "v_cvt_pk_bf16_f32 v58, v28, v29\n\t"
    "v_cvt_pk_bf16_f32 v59, v30, v31\n\t"
    "ds_write_b128 v4, v[56:59] offset:28672\n\t"
    "v_pk_fma_f32 v[32:33], v[32:33], v[104:105], v[104:105] op_sel:[0,0,1] op_sel_hi:[1,0,1]\n\t"
    "v_pk_fma_f32 v[34:35], v[34:35], v[104:105], v[104:105] op_sel:[0,0,1] op_sel_hi:[1,0,1]\n\t"
    "v_pk_fma_f32 v[36:37], v[36:37], v[104:105], v[104:105] op_sel:[0,0,1] op_sel_hi:[1,0,1]\n\t"
    "v_pk_fma_f32 v[38:39], v[38:39], v[104:105], v[104:105] op_sel:[0,0,1] op_sel_hi:[1,0,1]\n\t"
    "v_pk_fma_f32 v[32:33], v[32:33], v[144:145], v[200:201]\n\t"
    "v_pk_fma_f32 v[34:35], v[34:35], v[146:147], v[202:203]\n\t"
    "v_pk_fma_f32 v[36:37], v[36:37], v[148:149], v[204:205]\n\t"
    "v_pk_fma_f32 v[38:39], v[38:39], v[150:151], v[206:207]\n\t"
    "v_cvt_pk_bf16_f32 v60, v32, v33\n\t"
    "s_add_u32 m0, s43, 10240\n\t"
    "v_cvt_pk_bf16_f32 v61, v34, v35\n\t"
    "v_cvt_pk_bf16_f32 v62, v36, v37\n\t"
    "buffer_load_dwordx4 v2, s[20:23], s74 offen  lds\n\t"
    "v_cvt_pk_bf16_f32 v63, v38, v39\n\t"
    "ds_write_b128 v4, v[60:63] offset:29696\n\t"
    "v_pk_fma_f32 v[40:41], v[40:41], v[104:105], v[104:105] op_sel:[0,0,1] op_sel_hi:[1,0,1]\n\t"
    "v_pk_fma_f32 v[42:43], v[42:43], v[104:105], v[104:105] op_sel:[0,0,1] op_sel_hi:[1,0,1]\n\t"
    "v_pk_fma_f32 v[44:45], v[44:45], v[104:105], v[104:105] op_sel:[0,0,1] op_sel_hi:[1,0,1]\n\t"
    "v_pk_fma_f32 v[46:47], v[46:47], v[104:105], v[104:105] op_sel:[0,0,1] op_sel_hi:[1,0,1]\n\t"
    "v_pk_fma_f32 v[40:41], v[40:41], v[152:153], v[208:209]\n\t"
    "v_pk_fma_f32 v[42:43], v[42:43], v[154:155], v[210:211]\n\t"
    "s_add_u32 m0, s43, 11264\n\t"
    "v_pk_fma_f32 v[44:45], v[44:45], v[156:157], v[212:213]\n\t"
    "v_pk_fma_f32 v[46:47], v[46:47], v[158:159], v[214:215]\n\t"
    "buffer_load_dwordx4 v2, s[20:23], s75 offen  lds\n\t"
    "v_cvt_pk_bf16_f32 v64, v40, v41\n\t"
    "v_cvt_pk_bf16_f32 v65, v42, v43\n\t"
    "v_cvt_pk_bf16_f32 v66, v44, v45\n\t"
    "v_cvt_pk_bf16_f32 v67, v46, v47\n\t"
    "ds_write_b128 v4, v[64:67] offset:30720\n\t"
    "v_pk_fma_f32 v[48:49], v[48:49], v[104:105], v[104:105] op_sel:[0,0,1] op_sel_hi:[1,0,1]\n\t"
    "v_pk_fma_f32 v[50:51], v[50:51], v[104:105], v[104:105] op_sel:[0,0,1] op_sel_hi:[1,0,1]\n\t"
    "v_pk_fma_f32 v[52:53], v[52:53], v[104:105], v[104:105] op_sel:[0,0,1] op_sel_hi:[1,0,1]\n\t"
    "v_pk_fma_f32 v[54:55], v[54:55], v[104:105], v[104:105] op_sel:[0,0,1] op_sel_hi:[1,0,1]\n\t"
    "v_pk_fma_f32 v[48:49], v[48:49], v[160:161], v[216:217]\n\t"
    "v_pk_fma_f32 v[50:51], v[50:51], v[162:163], v[218:219]\n\t"
    "v_pk_fma_f32 v[52:53], v[52:53], v[164:165], v[220:221]\n\t"
    "s_waitcnt vmcnt(11)\n\t"
    "ds_read_b64_tr_b16 a[0:1], v5 offset:0\n\t"
    "v_pk_fma_f32 v[54:55], v[54:55], v[166:167], v[222:223]\n\t"
    "v_cvt_pk_bf16_f32 v68, v48, v49\n\t"
    "ds_read_b64_tr_b16 a[2:3], v5 offset:256\n\t"
    "v_cvt_pk_bf16_f32 v69, v50, v51\n\t"
    "v_cvt_pk_bf16_f32 v70, v52, v53\n\t"
    "ds_read_b64_tr_b16 a[56:57], v6 offset:0\n\t"
    "v_cvt_pk_bf16_f32 v71, v54, v55\n\t"
    "ds_write_b128 v4, v[68:71] offset:31744\n\t"
    "ds_read_b64_tr_b16 a[58:59], v6 offset:256\n\t"
    "v_pk_fma_f32 v[112:113], v[112:113], v[104:105], v[104:105] op_sel:[0,0,1] op_sel_hi:[1,0,1]\n\t"
    "v_pk_fma_f32 v[114:115], v[114:115], v[104:105], v[104:105] op_sel:[0,0,1] op_sel_hi:[1,0,1]\n\t"
    "v_pk_fma_f32 v[116:117], v[116:117], v[104:105], v[104:105] op_sel:[0,0,1] op_sel_hi:[1,0,1]\n\t"
    "v_pk_fma_f32 v[118:119], v[118:119], v[104:105], v[104:105] op_sel:[0,0,1] op_sel_hi:[1,0,1]\n\t"
    "v_pk_fma_f32 v[112:113], v[112:113], v[168:169], v[224:225]\n\t"
    "v_pk_fma_f32 v[114:115], v[114:115], v[170:171], v[226:227]\n\t"
    "s_waitcnt vmcnt(9)\n\t"
    "ds_read_b64_tr_b16 a[4:5], v5 offset:2048\n\t"
    "v_pk_fma_f32 v[116:117], v[116:117], v[172:173], v[228:229]\n\t"
    "v_pk_fma_f32 v[118:119], v[118:119], v[174:175], v[230:231]\n\t"
    "ds_read_b64_tr_b16 a[6:7], v5 offset:2304\n\t"
    "v_cvt_pk_bf16_f32 v72, v112, v113\n\t"
    "v_cvt_pk_bf16_f32 v73, v114, v115\n\t"
    "ds_read_b64_tr_b16 a[60:61], v6 offset:2048\n\t"
    "v_cvt_pk_bf16_f32 v74, v116, v117\n\t"
    "v_cvt_pk_bf16_f32 v75, v118, v119\n\t"
    "ds_read_b64_tr_b16 a[62:63], v6 offset:2304\n\t"
    "ds_write_b128 v4, v[72:75] offset:32768\n\t"
    "v_pk_fma_f32 v[120:121], v[120:121], v[104:105], v[104:105] op_sel:[0,0,1] op_sel_hi:[1,0,1]\n\t"
    "v_pk_fma_f32 v[122:123], v[122:123], v[104:105], v[104:105] op_sel:[0,0,1] op_sel_hi:[1,0,1]\n\t"
    "v_pk_fma_f32 v[124:125], v[124:125], v[104:105], v[104:105] op_sel:[0,0,1] op_sel_hi:[1,0,1]\n\t"
    "s_add_u32 m0, s43, 0\n\t"
    "v_pk_fma_f32 v[126:127], v[126:127], v[104:105], v[104:105] op_sel:[0,0,1] op_sel_hi:[1,0,1]\n\t"
    "v_pk_fma_f32 v[120:121], v[120:121], v[176:177], v[232:233]\n\t"
    "s_waitcnt lgkmcnt(5)\n\t"
    "buffer_load_dwordx4 v2, s[20:23], s76 offen  lds\n\t"
    "v_pk_fma_f32 v[122:123], v[122:123], v[178:179], v[234:235]\n\t"
    "v_pk_fma_f32 v[124:125], v[124:125], v[180:181], v[236:237]\n\t"
    "s_waitcnt vmcnt(8)\n\t"
    "ds_read_b64_tr_b16 a[8:9], v5 offset:4096\n\t"
    "v_pk_fma_f32 v[126:127], v[126:127], v[182:183], v[238:239]\n\t"
    "v_cvt_pk_bf16_f32 v76, v120, v121\n\t"
    "ds_read_b64_tr_b16 a[10:11], v5 offset:4352\n\t"
    "v_cvt_pk_bf16_f32 v77, v122, v123\n\t"
    "v_cvt_pk_bf16_f32 v78, v124, v125\n\t"
    "ds_read_b64_tr_b16 a[64:65], v6 offset:4096\n\t"
    "v_cvt_pk_bf16_f32 v79, v126, v127\n\t"
    "ds_write_b128 v4, v[76:79] offset:33792\n\t"
    "s_add_u32 m0, s43, 1024\n\t"
    "ds_read_b64_tr_b16 a[66:67], v6 offset:4352\n\t"
    "v_pk_fma_f32 v[84:85], v[84:85], v[104:105], v[104:105] op_sel:[0,0,1] op_sel_hi:[1,0,1]\n\t"
    "v_pk_fma_f32 v[86:87], v[86:87], v[104:105], v[104:105] op_sel:[0,0,1] op_sel_hi:[1,0,1]\n\t"
    "buffer_load_dwordx4 v2, s[20:23], s77 offen  lds\n\t"
    "v_pk_fma_f32 v[88:89], v[88:89], v[104:105], v[104:105] op_sel:[0,0,1] op_sel_hi:[1,0,1]\n\t"
    "v_pk_fma_f32 v[90:91], v[90:91], v[104:105], v[104:105] op_sel:[0,0,1] op_sel_hi:[1,0,1]\n\t"
    "v_pk_fma_f32 v[84:85], v[84:85], v[184:185], v[240:241]\n\t"
    "v_pk_fma_f32 v[86:87], v[86:87], v[186:187], v[242:243]\n\t"
    "v_pk_fma_f32 v[88:89], v[88:89], v[188:189], v[244:245]\n\t"
    "v_pk_fma_f32 v[90:91], v[90:91], v[190:191], v[246:247]\n\t"
    "v_cvt_pk_bf16_f32 v80, v84, v85\n\t"
    "v_cvt_pk_bf16_f32 v81, v86, v87\n\t"
    "s_add_u32 m0, s43, 2048\n\t"
    "v_cvt_pk_bf16_f32 v82, v88, v89\n\t"
    "v_cvt_pk_bf16_f32 v83, v90, v91\n\t"
    "s_waitcnt lgkmcnt(6)\n\t"
    "buffer_load_dwordx4 v2, s[20:23], s78 offen  lds\n\t"
    "ds_write_b128 v4, v[80:83] offset:34816\n\t"
    "s_waitcnt lgkmcnt(0)\n\t"
    "s_barrier\n\t"
    "s_add_u32 m0, s43, 3072\n\t"
    "ds_read_b128 v[56:59], v10 offset:0\n\t"
    "ds_read_b128 v[60:63], v11 offset:0\n\t"
    "ds_read_b128 v[64:67], v10 offset:28672\n\t"
    "ds_read_b128 v[68:71], v10 offset:1024\n\t"
    "ds_read_b128 v[72:75], v11 offset:28672\n\t"
    "ds_read_b128 v[76:79], v14 offset:0\n\t"
    "ds_read_b128 v[80:83], v11 offset:1024\n\t"
    "s_waitcnt lgkmcnt(6)\n\t"
    "v_mfma_f32_16x16x32_bf16 v[24:27], a[0:3], v[56:59], 0\n\t"
    "s_waitcnt vmcnt(7)\n\t"
    "ds_read_b128 v[84:87], v10 offset:29696\n\t"
    "ds_read_b64_tr_b16 a[12:13], v5 offset:6144\n\t"
    "v_mfma_f32_16x16x32_bf16 v[28:31], a[56:59], v[56:59], 0\n\t"
    "s_waitcnt lgkmcnt(7)\n\t"
    "ds_read_b128 v[88:91], v15 offset:0\n\t"
    "ds_read_b64_tr_b16 a[14:15], v5 offset:6400\n\t"
    "v_mfma_f32_16x16x32_bf16 v[24:27], a[4:7], v[60:63], v[24:27]\n\t"
    "buffer_load_dwordx4 v2, s[20:23], s79 offen  lds\n\t"
    "v_mfma_f32_16x16x32_bf16 v[28:31], a[60:63], v[60:63], v[28:31]\n\t"
    "s_waitcnt lgkmcnt(8)\n\t"
    "ds_read_b128 v[92:95], v8 offset:0\n\t"
    "s_add_u32 m0, s43, 4096\n\t"
    "v_mfma_f32_16x16x32_bf16 v[32:35], a[0:3], v[64:67], 0\n\t"
    "ds_read_b128 v[96:99], v10 offset:2048\n\t"
    "ds_read_b64_tr_b16 a[68:69], v6 offset:6144\n\t"
    "v_mfma_f32_16x16x32_bf16 v[36:39], a[56:59], v[64:67], 0\n\t"
    "s_waitcnt lgkmcnt(10)\n\t"
    "buffer_load_dwordx4 v2, s[20:23], s80 offen  lds\n\t"
    "v_mfma_f32_16x16x32_bf16 v[24:27], a[8:11], v[68:71], v[24:27]\n\t"
    "s_add_u32 m0, s43, 5120\n\t"
    "ds_read_b128 v[100:103], v11 offset:29696\n\t"
    "ds_read_b64_tr_b16 a[70:71], v6 offset:6400\n\t"
    "v_mfma_f32_16x16x32_bf16 v[28:31], a[64:67], v[68:71], v[28:31]\n\t"
    "s_waitcnt lgkmcnt(11)\n\t"
    "ds_read_b128 v[104:107], v14 offset:1024\n\t"
    "v_mfma_f32_16x16x32_bf16 v[32:35], a[4:7], v[72:75], v[32:35]\n\t"
    "s_waitcnt vmcnt(7)\n\t"
    "buffer_load_dwordx4 v2, s[20:23], s81 offen  lds\n\t"
    "v_mfma_f32_16x16x32_bf16 v[36:39], a[60:63], v[72:75], v[36:39]\n\t"
    "s_waitcnt lgkmcnt(11)\n\t"
    "ds_read_b128 v[108:111], v9 offset:0\n\t"
    "ds_read_b64_tr_b16 a[16:17], v5 offset:8192\n\t"
    "v_mfma_f32_16x16x32_bf16 v[40:43], a[0:3], v[76:79], 0\n\t"
    "ds_read_b64_tr_b16 a[18:19], v5 offset:8448\n\t"
    "ds_read_b64_tr_b16 a[72:73], v6 offset:8192\n\t"
    "v_mfma_f32_16x16x32_bf16 v[44:47], a[56:59], v[76:79], 0\n\t"
    "s_waitcnt lgkmcnt(10)\n\t"
    "ds_read_b64_tr_b16 a[74:75], v6 offset:8448\n\t"
    "v_mfma_f32_16x16x32_bf16 v[24:27], a[12:15], v[80:83], v[24:27]\n\t"
    "s_waitcnt lgkmcnt(6)\n\t"
    "s_add_u32 m0, s43, 6144\n\t"
    "v_mfma_f32_16x16x32_bf16 v[28:31], a[68:71], v[80:83], v[28:31]\n\t"
    "s_waitcnt vmcnt(6)\n\t"
    "buffer_load_dwordx4 v2, s[20:23], s82 offen  lds\n\t"
    "v_mfma_f32_16x16x32_bf16 v[32:35], a[8:11], v[84:87], v[32:35]\n\t"
    "s_add_u32 m0, s43, 7168\n\t"
    "ds_read_b128 v[56:59], v11 offset:2048\n\t"
    "ds_read_b64_tr_b16 a[20:21], v5 offset:10240\n\t"
    "v_mfma_f32_16x16x32_bf16 v[36:39], a[64:67], v[84:87], v[36:39]\n\t"
    "ds_read_b128 v[60:63], v10 offset:30720\n\t"
    "ds_read_b64_tr_b16 a[22:23], v5 offset:10496\n\t"
    "v_mfma_f32_16x16x32_bf16 v[40:43], a[4:7], v[88:91], v[40:43]\n\t"
    "buffer_load_dwordx4 v2, s[20:23], s83 offen  lds\n\t"
    "v_mfma_f32_16x16x32_bf16 v[44:47], a[60:63], v[88:91], v[44:47]\n\t"
    "ds_read_b128 v[64:67], v15 offset:1024\n\t"
    "ds_read_b64_tr_b16 a[76:77], v6 offset:10240\n\t"
    "v_mfma_f32_16x16x32_bf16 v[48:51], a[0:3], v[92:95], 0\n\t"
    "ds_read_b128 v[68:71], v8 offset:1024\n\t"
    "ds_read_b64_tr_b16 a[78:79], v6 offset:10496\n\t"
    "v_mfma_f32_16x16x32_bf16 v[52:55], a[56:59], v[92:95], 0\n\t"
    "s_waitcnt lgkmcnt(10)\n\t"
    "s_add_u32 m0, s43, 8192\n\t"
    "v_mfma_f32_16x16x32_bf16 v[24:27], a[16:19], v[96:99], v[24:27]\n\t"
    "s_waitcnt lgkmcnt(8)\n\t"
    "ds_read_b128 v[72:75], v10 offset:3072\n\t"
    "v_mfma_f32_16x16x32_bf16 v[28:31], a[72:75], v[96:99], v[28:31]\n\t"
    "s_waitcnt vmcnt(6)\n\t"
    "ds_read_b128 v[76:79], v11 offset:30720\n\t"
    "ds_read_b64_tr_b16 a[24:25], v5 offset:0\n\t"
    "v_mfma_f32_16x16x32_bf16 v[32:35], a[12:15], v[100:103], v[32:35]\n\t"
    "buffer_load_dwordx4 v2, s[20:23], s84 offen  lds\n\t"
    "v_mfma_f32_16x16x32_bf16 v[36:39], a[68:71], v[100:103], v[36:39]\n\t"
    "s_add_u32 m0, s43, 9216\n\t"
    "ds_read_b128 v[80:83], v14 offset:2048\n\t"
    "ds_read_b64_tr_b16 a[26:27], v5 offset:256\n\t"
    "v_mfma_f32_16x16x32_bf16 v[40:43], a[8:11], v[104:107], v[40:43]\n\t"
    "buffer_load_dwordx4 v2, s[20:23], s85 offen  lds\n\t"
    "v_mfma_f32_16x16x32_bf16 v[44:47], a[64:67], v[104:107], v[44:47]\n\t"
    "ds_read_b64_tr_b16 a[80:81], v6 offset:0\n\t"
    "ds_read_b64_tr_b16 a[82:83], v6 offset:256\n\t"
    "v_mfma_f32_16x16x32_bf16 v[48:51], a[4:7], v[108:111], v[48:51]\n\t"
    "s_waitcnt vmcnt(6) lgkmcnt(14)\n\t"
    "ds_read_b64_tr_b16 a[28:29], v5 offset:2048\n\t"
    "v_mfma_f32_16x16x32_bf16 v[52:55], a[60:63], v[108:111], v[52:55]\n\t"
    "s_waitcnt lgkmcnt(12)\n\t"
    "ds_read_b64_tr_b16 a[30:31], v5 offset:2304\n\t"
    "ds_read_b64_tr_b16 a[84:85], v6 offset:2048\n\t"
    "v_mfma_f32_16x16x32_bf16 v[24:27], a[20:23], v[56:59], v[24:27]\n\t"
    "s_waitcnt lgkmcnt(10)\n\t"
    "ds_read_b128 v[84:87], v9 offset:1024\n\t"
    "s_add_u32 m0, s43, 10240\n\t"
    "v_mfma_f32_16x16x32_bf16 v[28:31], a[76:79], v[56:59], v[28:31]\n\t"
    "ds_read_b128 v[88:91], v11 offset:3072\n\t"
    "ds_read_b64_tr_b16 a[86:87], v6 offset:2304\n\t"
    "v_mfma_f32_16x16x32_bf16 v[32:35], a[16:19], v[60:63], v[32:35]\n\t"
    "buffer_load_dwordx4 v2, s[20:23], s86 offen  lds\n\t"
    "v_mfma_f32_16x16x32_bf16 v[36:39], a[72:75], v[60:63], v[36:39]\n\t"
    "s_add_u32 m0, s43, 11264\n\t"
    "ds_read_b128 v[92:95], v10 offset:31744\n\t"
    "v_mfma_f32_16x16x32_bf16 v[40:43], a[12:15], v[64:67], v[40:43]\n\t"
    "ds_read_b128 v[96:99], v15 offset:2048\n\t"
    "v_mfma_f32_16x16x32_bf16 v[44:47], a[68:71], v[64:67], v[44:47]\n\t"
    "buffer_load_dwordx4 v2, s[20:23], s87 offen  lds\n\t"
    "v_mfma_f32_16x16x32_bf16 v[48:51], a[8:11], v[68:71], v[48:51]\n\t"
    "s_waitcnt lgkmcnt(14)\n\t"
    "ds_read_b128 v[100:103], v8 offset:2048\n\t"
    "v_mfma_f32_16x16x32_bf16 v[52:55], a[64:67], v[68:71], v[52:55]\n\t"
    "s_waitcnt lgkmcnt(11)\n\t"
    "ds_read_b128 v[104:107], v10 offset:4096\n\t"
    "v_mfma_f32_16x16x32_bf16 v[24:27], a[24:27], v[72:75], v[24:27]\n\t"
    "s_waitcnt lgkmcnt(10)\n\t"
    "s_add_u32 m0, s43, 0\n\t"
    "v_mfma_f32_16x16x32_bf16 v[28:31], a[80:83], v[72:75], v[28:31]\n\t"
    "s_waitcnt vmcnt(6)\n\t"
    "ds_read_b128 v[108:111], v11 offset:31744\n\t"
    "ds_read_b64_tr_b16 a[32:33], v5 offset:4096\n\t"
    "v_mfma_f32_16x16x32_bf16 v[32:35], a[20:23], v[76:79], v[32:35]\n\t"
    "buffer_load_dwordx4 v2, s[20:23], s88 offen  lds\n\t"
    "v_mfma_f32_16x16x32_bf16 v[36:39], a[76:79], v[76:79], v[36:39]\n\t"
    "s_add_u32 m0, s43, 1024\n\t"
    "ds_read_b64_tr_b16 a[34:35], v5 offset:4352\n\t"
    "ds_read_b64_tr_b16 a[88:89], v6 offset:4096\n\t"
    "v_mfma_f32_16x16x32_bf16 v[40:43], a[16:19], v[80:83], v[40:43]\n\t"
    "buffer_load_dwordx4 v2, s[20:23], s89 offen  lds\n\t"
    "v_mfma_f32_16x16x32_bf16 v[44:47], a[72:75], v[80:83], v[44:47]\n\t"
    "s_waitcnt lgkmcnt(10)\n\t"
    "ds_read_b64_tr_b16 a[90:91], v6 offset:4352\n\t"
    "v_mfma_f32_16x16x32_bf16 v[48:51], a[12:15], v[84:87], v[48:51]\n\t"
    "ds_read_b128 v[56:59], v14 offset:3072\n\t"
    "v_mfma_f32_16x16x32_bf16 v[52:55], a[68:71], v[84:87], v[52:55]\n\t"
    "s_waitcnt lgkmcnt(11)\n\t"
    "ds_read_b128 v[60:63], v9 offset:2048\n\t"
    "v_mfma_f32_16x16x32_bf16 v[24:27], a[28:31], v[88:91], v[24:27]\n\t"
    "s_waitcnt lgkmcnt(11)\n\t"
    "v_mfma_f32_16x16x32_bf16 v[28:31], a[84:87], v[88:91], v[28:31]\n\t"
    "s_waitcnt lgkmcnt(10)\n\t"
    "ds_read_b128 v[64:67], v11 offset:4096\n\t"
    "s_add_u32 m0, s43, 2048\n\t"
    "v_mfma_f32_16x16x32_bf16 v[32:35], a[24:27], v[92:95], v[32:35]\n\t"
    "s_waitcnt vmcnt(6)\n\t"
    "ds_read_b128 v[68:71], v10 offset:32768\n\t"
    "ds_read_b64_tr_b16 a[36:37], v5 offset:6144\n\t"
    "v_mfma_f32_16x16x32_bf16 v[36:39], a[80:83], v[92:95], v[36:39]\n\t"
    "s_waitcnt lgkmcnt(12)\n\t"
    "buffer_load_dwordx4 v2, s[20:23], s90 offen  lds\n\t"
    "v_mfma_f32_16x16x32_bf16 v[40:43], a[20:23], v[96:99], v[40:43]\n\t"
    "s_add_u32 m0, s43, 3072\n\t"
    "ds_read_b128 v[72:75], v15 offset:3072\n\t"
    "ds_read_b64_tr_b16 a[38:39], v5 offset:6400\n\t"
    "v_mfma_f32_16x16x32_bf16 v[44:47], a[76:79], v[96:99], v[44:47]\n\t"
    "s_waitcnt lgkmcnt(13)\n\t"
    "ds_read_b128 v[76:79], v8 offset:3072\n\t"
    "ds_read_b64_tr_b16 a[92:93], v6 offset:6144\n\t"
    "v_mfma_f32_16x16x32_bf16 v[48:51], a[16:19], v[100:103], v[48:51]\n\t"
    "s_waitcnt lgkmcnt(14)\n\t"
    "buffer_load_dwordx4 v2, s[20:23], s91 offen  lds\n\t"
    "v_mfma_f32_16x16x32_bf16 v[52:55], a[72:75], v[100:103], v[52:55]\n\t"
    "s_waitcnt lgkmcnt(11)\n\t"
    "ds_read_b128 v[80:83], v10 offset:5120\n\t"
    "ds_read_b64_tr_b16 a[94:95], v6 offset:6400\n\t"
    "v_mfma_f32_16x16x32_bf16 v[24:27], a[32:35], v[104:107], v[24:27]\n\t"
    "s_waitcnt lgkmcnt(11)\n\t"
    "v_mfma_f32_16x16x32_bf16 v[28:31], a[88:91], v[104:107], v[28:31]\n\t"
    "s_waitcnt vmcnt(6)\n\t"
    "v_add_u32 v2, 256, v2\n\t"
    "s_add_u32 m0, s43, 4096\n\t"
    "v_mfma_f32_16x16x32_bf16 v[32:35], a[28:31], v[108:111], v[32:35]\n\t"
    "buffer_load_dwordx4 v2, s[20:23], s64 offen  lds\n\t"
    "v_mfma_f32_16x16x32_bf16 v[36:39], a[84:87], v[108:111], v[36:39]\n\t"
    "s_waitcnt lgkmcnt(10)\n\t"
    "ds_read_b64_tr_b16 a[40:41], v5 offset:8192\n\t"
    "ds_read_b64_tr_b16 a[42:43], v5 offset:8448\n\t"
    "v_mfma_f32_16x16x32_bf16 v[40:43], a[24:27], v[56:59], v[40:43]\n\t"
    "s_add_u32 m0, s43, 5120\n\t"
    "ds_read_b128 v[84:87], v11 offset:32768\n\t"
    "ds_read_b64_tr_b16 a[96:97], v6 offset:8192\n\t"
    "v_mfma_f32_16x16x32_bf16 v[44:47], a[80:83], v[56:59], v[44:47]\n\t"
    "s_waitcnt lgkmcnt(13)\n\t"
    "ds_read_b128 v[88:91], v14 offset:4096\n\t"
    "ds_read_b64_tr_b16 a[98:99], v6 offset:8448\n\t"
    "v_mfma_f32_16x16x32_bf16 v[48:51], a[20:23], v[60:63], v[48:51]\n\t"
    "buffer_load_dwordx4 v2, s[20:23], s65 offen  lds\n\t"
    "v_mfma_f32_16x16x32_bf16 v[52:55], a[76:79], v[60:63], v[52:55]\n\t"
    "s_waitcnt lgkmcnt(10)\n\t"
    "ds_read_b128 v[92:95], v9 offset:3072\n\t"
    "v_mfma_f32_16x16x32_bf16 v[24:27], a[36:39], v[64:67], v[24:27]\n\t"
    "s_waitcnt lgkmcnt(7)\n\t"
    "ds_read_b128 v[96:99], v11 offset:5120\n\t"
    "v_mfma_f32_16x16x32_bf16 v[28:31], a[92:95], v[64:67], v[28:31]\n\t"
    "s_add_u32 m0, s43, 6144\n\t"
    "s_waitcnt vmcnt(6)\n\t"
    "ds_read_b64_tr_b16 a[44:45], v5 offset:10240\n\t"
    "v_mfma_f32_16x16x32_bf16 v[32:35], a[32:35], v[68:71], v[32:35]\n\t"
    "ds_read_b128 v[100:103], v10 offset:33792\n\t"
    "ds_read_b64_tr_b16 a[46:47], v5 offset:10496\n\t"
    "v_mfma_f32_16x16x32_bf16 v[36:39], a[88:91], v[68:71], v[36:39]\n\t"
    "ds_read_b128 v[104:107], v15 offset:4096\n\t"
    "ds_read_b64_tr_b16 a[100:101], v6 offset:10240\n\t"
    "v_mfma_f32_16x16x32_bf16 v[40:43], a[28:31], v[72:75], v[40:43]\n\t"
    "buffer_load_dwordx4 v2, s[20:23], s66 offen  lds\n\t"
    "v_mfma_f32_16x16x32_bf16 v[44:47], a[84:87], v[72:75], v[44:47]\n\t"
    "s_add_u32 m0, s43, 7168\n\t"
    "ds_read_b128 v[108:111], v8 offset:4096\n\t"
    "ds_read_b64_tr_b16 a[102:103], v6 offset:10496\n\t"
    "v_mfma_f32_16x16x32_bf16 v[48:51], a[24:27], v[76:79], v[48:51]\n\t"
    "buffer_load_dwordx4 v2, s[20:23], s67 offen  lds\n\t"
    "v_mfma_f32_16x16x32_bf16 v[52:55], a[80:83], v[76:79], v[52:55]\n\t"
    "s_waitcnt lgkmcnt(13)\n\t"
    "v_mfma_f32_16x16x32_bf16 v[24:27], a[40:43], v[80:83], v[24:27]\n\t"
    "s_waitcnt lgkmcnt(9)\n\t"
    "v_mfma_f32_16x16x32_bf16 v[28:31], a[96:99], v[80:83], v[28:31]\n\t"
    "s_waitcnt vmcnt(6)\n\t"
    "ds_read_b64_tr_b16 a[48:49], v5 offset:0\n\t"
    "ds_read_b64_tr_b16 a[50:51], v5 offset:256\n\t"
    "v_mfma_f32_16x16x32_bf16 v[32:35], a[36:39], v[84:87], v[32:35]\n\t"
    "s_add_u32 m0, s43, 8192\n\t"
    "ds_read_b128 v[56:59], v10 offset:6144\n\t"
    "ds_read_b64_tr_b16 a[104:105], v6 offset:0\n\t"
    "v_mfma_f32_16x16x32_bf16 v[36:39], a[92:95], v[84:87], v[36:39]\n\t"
    "ds_read_b128 v[60:63], v11 offset:33792\n\t"
    "ds_read_b64_tr_b16 a[106:107], v6 offset:256\n\t"
    "v_mfma_f32_16x16x32_bf16 v[40:43], a[32:35], v[88:91], v[40:43]\n\t"
    "buffer_load_dwordx4 v2, s[20:23], s68 offen  lds\n\t"
    "v_mfma_f32_16x16x32_bf16 v[44:47], a[88:91], v[88:91], v[44:47]\n\t"
    "s_waitcnt lgkmcnt(14)\n\t"
    "ds_read_b128 v[64:67], v14 offset:5120\n\t"
    "s_add_u32 m0, s43, 9216\n\t"
    "v_mfma_f32_16x16x32_bf16 v[48:51], a[28:31], v[92:95], v[48:51]\n\t"
    "s_waitcnt lgkmcnt(14)\n\t"
    "ds_read_b128 v[68:71], v9 offset:4096\n\t"
    "v_mfma_f32_16x16x32_bf16 v[52:55], a[84:87], v[92:95], v[52:55]\n\t"
    "s_waitcnt lgkmcnt(12)\n\t"
    "buffer_load_dwordx4 v2, s[20:23], s69 offen  lds\n\t"
    "v_mfma_f32_16x16x32_bf16 v[24:27], a[44:47], v[96:99], v[24:27]\n\t"
    "s_waitcnt lgkmcnt(8)\n\t"
    "ds_read_b128 v[72:75], v11 offset:6144\n\t"
    "v_mfma_f32_16x16x32_bf16 v[28:31], a[100:103], v[96:99], v[28:31]\n\t"
    "s_waitcnt vmcnt(6)\n\t"
    "ds_read_b128 v[76:79], v10 offset:34816\n\t"
    "ds_read_b64_tr_b16 a[52:53], v5 offset:2048\n\t"
    "v_mfma_f32_16x16x32_bf16 v[32:35], a[40:43], v[100:103], v[32:35]\n\t"
    "s_add_u32 m0, s43, 10240\n\t"
    "ds_read_b64_tr_b16 a[54:55], v5 offset:2304\n\t"
    "ds_read_b64_tr_b16 a[108:109], v6 offset:2048\n\t"
    "v_mfma_f32_16x16x32_bf16 v[36:39], a[96:99], v[100:103], v[36:39]\n\t"
    "ds_read_b128 v[80:83], v15 offset:5120\n\t"
    "ds_read_b64_tr_b16 a[110:111], v6 offset:2304\n\t"
    "v_mfma_f32_16x16x32_bf16 v[40:43], a[36:39], v[104:107], v[40:43]\n\t"
    "buffer_load_dwordx4 v2, s[20:23], s70 offen  lds\n\t"
    "v_mfma_f32_16x16x32_bf16 v[44:47], a[92:95], v[104:107], v[44:47]\n\t"
    "s_add_u32 m0, s43, 11264\n\t"
    "v_mfma_f32_16x16x32_bf16 v[48:51], a[32:35], v[108:111], v[48:51]\n\t"
    "buffer_load_dwordx4 v2, s[20:23], s71 offen  lds\n\t"
    "v_mfma_f32_16x16x32_bf16 v[52:55], a[88:91], v[108:111], v[52:55]\n\t"
    "s_waitcnt lgkmcnt(12)\n\t"
    "v_mfma_f32_16x16x32_bf16 v[24:27], a[48:51], v[56:59], v[24:27]\n\t"
    "s_waitcnt lgkmcnt(9)\n\t"
    "ds_read_b128 v[84:87], v8 offset:5120\n\t"
    "v_mfma_f32_16x16x32_bf16 v[28:31], a[104:107], v[56:59], v[28:31]\n\t"
    "ds_read_b128 v[88:91], v11 offset:34816\n\t"
    "v_mfma_f32_16x16x32_bf16 v[32:35], a[44:47], v[60:63], v[32:35]\n\t"
    "v_mfma_f32_16x16x32_bf16 v[36:39], a[100:103], v[60:63], v[36:39]\n\t"
    "s_waitcnt lgkmcnt(10)\n\t"
    "ds_read_b128 v[92:95], v14 offset:6144\n\t"
    "s_add_u32 m0, s43, 0\n\t"
    "v_mfma_f32_16x16x32_bf16 v[40:43], a[40:43], v[64:67], v[40:43]\n\t"
    "ds_read_b128 v[96:99], v9 offset:5120\n\t"
    "v_mfma_f32_16x16x32_bf16 v[44:47], a[96:99], v[64:67], v[44:47]\n\t"
    "s_waitcnt lgkmcnt(11)\n\t"
    "buffer_load_dwordx4 v2, s[20:23], s72 offen  lds\n\t"
    "v_mfma_f32_16x16x32_bf16 v[48:51], a[36:39], v[68:71], v[48:51]\n\t"
    "s_add_u32 m0, s43, 1024\n\t"
    "ds_read_b128 v[100:103], v15 offset:6144\n\t"
    "v_mfma_f32_16x16x32_bf16 v[52:55], a[92:95], v[68:71], v[52:55]\n\t"
    "s_waitcnt lgkmcnt(8)\n\t"
    "ds_read_b128 v[104:107], v8 offset:6144\n\t"
    "v_mfma_f32_16x16x32_bf16 v[24:27], a[52:55], v[72:75], v[24:27]\n\t"
    "s_waitcnt lgkmcnt(6)\n\t"
    "buffer_load_dwordx4 v2, s[20:23], s73 offen  lds\n\t"
    "v_mfma_f32_16x16x32_bf16 v[28:31], a[108:111], v[72:75], v[28:31]\n\t"
    "s_waitcnt vmcnt(8)\n\t"
    "ds_read_b128 v[108:111], v9 offset:6144\n\t"
    "ds_read_b64_tr_b16 a[112:113], v5 offset:4096\n\t"
    "v_mfma_f32_16x16x32_bf16 v[32:35], a[48:51], v[76:79], v[32:35]\n\t"
    "ds_read_b64_tr_b16 a[114:115], v5 offset:4352\n\t"
    "ds_read_b64_tr_b16 a[168:169], v6 offset:4096\n\t"
    "v_mfma_f32_16x16x32_bf16 v[36:39], a[104:107], v[76:79], v[36:39]\n\t"
    "s_add_u32 m0, s43, 2048\n\t"
    "ds_read_b64_tr_b16 a[170:171], v6 offset:4352\n\t"
    "v_mfma_f32_16x16x32_bf16 v[40:43], a[44:47], v[80:83], v[40:43]\n\t"
    "s_mov_b64 vcc, s[46:47]\n\t"
    "buffer_load_dwordx4 v2, s[20:23], s74 offen  lds\n\t"
    "v_mfma_f32_16x16x32_bf16 v[44:47], a[100:103], v[80:83], v[44:47]\n\t"
    "s_waitcnt lgkmcnt(10)\n\t"
    "v_cvt_pk_bf16_f32 v20, v24, v25\n\t"
    "v_cvt_pk_bf16_f32 v21, v26, v27\n\t"
    "v_mfma_f32_16x16x32_bf16 v[48:51], a[40:43], v[84:87], v[48:51]\n\t"
    "s_add_u32 m0, s43, 3072\n\t"
    "ds_read_b128 v[56:59], v8 offset:28672\n\t"
    "v_cvt_pk_bf16_f32 v22, v28, v29\n\t"
    "v_mfma_f32_16x16x32_bf16 v[52:55], a[96:99], v[84:87], v[52:55]\n\t"
    "s_waitcnt lgkmcnt(10)\n\t"
    "ds_read_b128 v[60:63], v9 offset:28672\n\t"
    "v_cvt_pk_bf16_f32 v23, v30, v31\n\t"
    "v_mfma_f32_16x16x32_bf16 v[32:35], a[52:55], v[88:91], v[32:35]\n\t"
    "s_waitcnt vmcnt(7)\n\t"
    "buffer_load_dwordx4 v2, s[20:23], s75 offen  lds\n\t"
    "v_mfma_f32_16x16x32_bf16 v[36:39], a[108:111], v[88:91], v[36:39]\n\t"
    "s_waitcnt lgkmcnt(10)\n\t"
    "ds_read_b128 v[64:67], v8 offset:29696\n\t"
    "v_cndmask_b32_dpp v112, v22, v20, vcc quad_perm:[1,0,3,2] row_mask:0xf bank_mask:0xf\n\t"
    "v_mfma_f32_16x16x32_bf16 v[40:43], a[48:51], v[92:95], v[40:43]\n\t"
    "ds_read_b128 v[68:71], v9 offset:29696\n\t"
    "v_cndmask_b32_dpp v113, v23, v21, vcc quad_perm:[1,0,3,2] row_mask:0xf bank_mask:0xf\n\t"
    "v_mfma_f32_16x16x32_bf16 v[44:47], a[104:107], v[92:95], v[44:47]\n\t"
    "s_waitcnt lgkmcnt(11)\n\t"
    "s_add_u32 m0, s43, 4096\n\t"
    "s_mov_b64 vcc, s[34:35]\n\t"
    "v_mfma_f32_16x16x32_bf16 v[48:51], a[44:47], v[96:99], v[48:51]\n\t"
    "ds_read_b128 v[72:75], v8 offset:30720\n\t"
    "v_cndmask_b32_dpp v114, v20, v22, vcc quad_perm:[1,0,3,2] row_mask:0xf bank_mask:0xf\n\t"
    "v_mfma_f32_16x16x32_bf16 v[52:55], a[100:103], v[96:99], v[52:55]\n\t"
    "s_waitcnt lgkmcnt(11)\n\t"
    "ds_read_b128 v[76:79], v9 offset:30720\n\t"
    "v_cndmask_b32_dpp v115, v21, v23, vcc quad_perm:[1,0,3,2] row_mask:0xf bank_mask:0xf\n\t"
    "v_mfma_f32_16x16x32_bf16 v[40:43], a[52:55], v[100:103], v[40:43]\n\t"
    "s_waitcnt lgkmcnt(6)\n\t"
    "buffer_load_dwordx4 v2, s[20:23], s76 offen  lds\n\t"
    "v_mfma_f32_16x16x32_bf16 v[44:47], a[108:111], v[100:103], v[44:47]\n\t"
    "s_add_u32 m0, s43, 5120\n\t"
    "ds_read_b128 v[80:83], v8 offset:31744\n\t"
    "v_permlane16_swap_b32 v112, v114\n\t"
    "v_mfma_f32_16x16x32_bf16 v[48:51], a[48:51], v[104:107], v[48:51]\n\t"
    "buffer_load_dwordx4 v2, s[20:23], s77 offen  lds\n\t"
    "v_mfma_f32_16x16x32_bf16 v[52:55], a[104:107], v[104:107], v[52:55]\n\t"
    "v_permlane16_swap_b32 v113, v115\n\t"
    "v_cvt_pk_bf16_f32 v20, v32, v33\n\t"
    "v_mfma_f32_16x16x32_bf16 v[48:51], a[52:55], v[108:111], v[48:51]\n\t"
    "v_cvt_pk_bf16_f32 v21, v34, v35\n\t"
    "v_cvt_pk_bf16_f32 v22, v36, v37\n\t"
    "v_mfma_f32_16x16x32_bf16 v[52:55], a[108:111], v[108:111], v[52:55]\n\t"
    "s_waitcnt lgkmcnt(5)\n\t"
    "v_cvt_pk_bf16_f32 v23, v38, v39\n\t"
    "v_cndmask_b32_dpp v118, v20, v22, vcc quad_perm:[1,0,3,2] row_mask:0xf bank_mask:0xf\n\t"
    "v_mfma_f32_16x16x32_bf16 v[24:27], a[0:3], v[56:59], 0\n\t"
    "ds_read_b128 v[84:87], v9 offset:31744\n\t"
    "v_cndmask_b32_dpp v119, v21, v23, vcc quad_perm:[1,0,3,2] row_mask:0xf bank_mask:0xf\n\t"
    "v_mfma_f32_16x16x32_bf16 v[24:27], a[4:7], v[60:63], v[24:27]\n\t"
    "s_waitcnt lgkmcnt(4)\n\t"
    "ds_read_b128 v[88:91], v8 offset:32768\n\t"
    "s_mov_b64 vcc, s[46:47]\n\t"
    "v_mfma_f32_16x16x32_bf16 v[24:27], a[8:11], v[64:67], v[24:27]\n\t"
    "buffer_store_dwordx4 v[112:115], v17, s[24:27], s64 offen nt\n\t"
    "v_mfma_f32_16x16x32_bf16 v[24:27], a[12:15], v[68:71], v[24:27]\n\t"
    "s_waitcnt lgkmcnt(3)\n\t"
    "ds_read_b128 v[92:95], v9 offset:32768\n\t"
    "v_cndmask_b32_dpp v116, v22, v20, vcc quad_perm:[1,0,3,2] row_mask:0xf bank_mask:0xf\n\t"
    "v_mfma_f32_16x16x32_bf16 v[24:27], a[16:19], v[72:75], v[24:27]\n\t"
    "ds_read_b128 v[96:99], v8 offset:33792\n\t"
    "v_cndmask_b32_dpp v117, v23, v21, vcc quad_perm:[1,0,3,2] row_mask:0xf bank_mask:0xf\n\t"
    "v_mfma_f32_16x16x32_bf16 v[24:27], a[20:23], v[76:79], v[24:27]\n\t"
    "s_waitcnt lgkmcnt(4)\n\t"
    "v_permlane16_swap_b32 v116, v118\n\t"
    "v_permlane16_swap_b32 v117, v119\n\t"
    "v_mfma_f32_16x16x32_bf16 v[24:27], a[24:27], v[80:83], v[24:27]\n\t"
    "ds_read_b128 v[100:103], v9 offset:33792\n\t"
    "v_cvt_pk_bf16_f32 v20, v40, v41\n\t"
    "v_mfma_f32_16x16x32_bf16 v[28:31], a[56:59], v[56:59], 0\n\t"
    "ds_read_b128 v[104:107], v8 offset:34816\n\t"
    "v_cvt_pk_bf16_f32 v21, v42, v43\n\t"
    "v_mfma_f32_16x16x32_bf16 v[28:31], a[60:63], v[60:63], v[28:31]\n\t"
    "v_cvt_pk_bf16_f32 v22, v44, v45\n\t"
    "v_cvt_pk_bf16_f32 v23, v46, v47\n\t"
    "v_mfma_f32_16x16x32_bf16 v[28:31], a[64:67], v[64:67], v[28:31]\n\t"
    "ds_read_b128 v[108:111], v9 offset:34816\n\t"
    "v_cndmask_b32_dpp v120, v22, v20, vcc quad_perm:[1,0,3,2] row_mask:0xf bank_mask:0xf\n\t"
    "v_mfma_f32_16x16x32_bf16 v[28:31], a[68:71], v[68:71], v[28:31]\n\t"
    "v_cndmask_b32_dpp v121, v23, v21, vcc quad_perm:[1,0,3,2] row_mask:0xf bank_mask:0xf\n\t"
    "s_mov_b64 vcc, s[34:35]\n\t"
    "v_mfma_f32_16x16x32_bf16 v[28:31], a[72:75], v[72:75], v[28:31]\n\t"
    "buffer_store_dwordx4 v[116:119], v17, s[24:27], s66 offen nt\n\t"
    "v_mfma_f32_16x16x32_bf16 v[28:31], a[76:79], v[76:79], v[28:31]\n\t"
    "v_cndmask_b32_dpp v122, v20, v22, vcc quad_perm:[1,0,3,2] row_mask:0xf bank_mask:0xf\n\t"
    "v_cndmask_b32_dpp v123, v21, v23, vcc quad_perm:[1,0,3,2] row_mask:0xf bank_mask:0xf\n\t"
    "v_mfma_f32_16x16x32_bf16 v[28:31], a[80:83], v[80:83], v[28:31]\n\t"
    "s_waitcnt lgkmcnt(5)\n\t"
    "v_permlane16_swap_b32 v120, v122\n\t"
    "v_permlane16_swap_b32 v121, v123\n\t"
    "v_mfma_f32_16x16x32_bf16 v[24:27], a[28:31], v[84:87], v[24:27]\n\t"
    "ds_read_b128 v[56:59], v12 offset:0\n\t"
    "v_cvt_pk_bf16_f32 v20, v48, v49\n\t"
    "v_mfma_f32_16x16x32_bf16 v[24:27], a[32:35], v[88:91], v[24:27]\n\t"
    "s_waitcnt lgkmcnt(4)\n\t"
    "ds_read_b128 v[60:63], v13 offset:0\n\t"
    "v_cvt_pk_bf16_f32 v21, v50, v51\n\t"
    "v_mfma_f32_16x16x32_bf16 v[24:27], a[36:39], v[92:95], v[24:27]\n\t"
    "v_cvt_pk_bf16_f32 v22, v52, v53\n\t"
    "v_cvt_pk_bf16_f32 v23, v54, v55\n\t"
    "v_mfma_f32_16x16x32_bf16 v[24:27], a[40:43], v[96:99], v[24:27]\n\t"
    "s_waitcnt lgkmcnt(3)\n\t"
    "ds_read_b128 v[64:67], v12 offset:1024\n\t"
    "v_cndmask_b32_dpp v126, v20, v22, vcc quad_perm:[1,0,3,2] row_mask:0xf bank_mask:0xf\n\t"
    "v_mfma_f32_16x16x32_bf16 v[24:27], a[44:47], v[100:103], v[24:27]\n\t"
    "ds_read_b128 v[68:71], v13 offset:1024\n\t"
    "v_cndmask_b32_dpp v127, v21, v23, vcc quad_perm:[1,0,3,2] row_mask:0xf bank_mask:0xf\n\t"
    "v_mfma_f32_16x16x32_bf16 v[24:27], a[48:51], v[104:107], v[24:27]\n\t"
    "s_waitcnt lgkmcnt(4)\n\t"
    "s_mov_b64 vcc, s[46:47]\n\t"
    "v_cndmask_b32_dpp v124, v22, v20, vcc quad_perm:[1,0,3,2] row_mask:0xf bank_mask:0xf\n\t"
    "v_mfma_f32_16x16x32_bf16 v[24:27], a[52:55], v[108:111], v[24:27]\n\t"
    "ds_read_b128 v[72:75], v12 offset:2048\n\t"
    "v_cndmask_b32_dpp v125, v23, v21, vcc quad_perm:[1,0,3,2] row_mask:0xf bank_mask:0xf\n\t"
    "v_mfma_f32_16x16x32_bf16 v[28:31], a[84:87], v[84:87], v[28:31]\n\t"
    "ds_read_b128 v[76:79], v13 offset:2048\n\t"
    "v_permlane16_swap_b32 v124, v126\n\t"
    "v_mfma_f32_16x16x32_bf16 v[28:31], a[88:91], v[88:91], v[28:31]\n\t"
    "buffer_store_dwordx4 v[120:123], v17, s[24:27], s68 offen nt\n\t"
    "v_mfma_f32_16x16x32_bf16 v[28:31], a[92:95], v[92:95], v[28:31]\n\t"
    "ds_read_b128 v[80:83], v12 offset:3072\n\t"
    "v_permlane16_swap_b32 v125, v127\n\t"
    "v_mfma_f32_16x16x32_bf16 v[28:31], a[96:99], v[96:99], v[28:31]\n\t"
    "ds_read_b64_tr_b16 a[116:117], v5 offset:6144\n\t"
    "ds_read_b64_tr_b16 a[118:119], v5 offset:6400\n\t"
    "v_mfma_f32_16x16x32_bf16 v[28:31], a[100:103], v[100:103], v[28:31]\n\t"
    "ds_read_b64_tr_b16 a[172:173], v6 offset:6144\n\t"
    "ds_read_b64_tr_b16 a[174:175], v6 offset:6400\n\t"
    "v_mfma_f32_16x16x32_bf16 v[28:31], a[104:107], v[104:107], v[28:31]\n\t"
    "s_waitcnt vmcnt(11)\n\t"
    "ds_read_b64_tr_b16 a[120:121], v5 offset:8192\n\t"
    "ds_read_b64_tr_b16 a[122:123], v5 offset:8448\n\t"
    "v_mfma_f32_16x16x32_bf16 v[28:31], a[108:111], v[108:111], v[28:31]\n\t"
    "s_waitcnt lgkmcnt(11)\n\t"
    "ds_read_b64_tr_b16 a[176:177], v6 offset:8192\n\t"
    "ds_read_b64_tr_b16 a[178:179], v6 offset:8448\n\t"
    "v_mfma_f32_16x16x32_bf16 v[32:35], a[0:3], v[56:59], 0\n\t"
    "s_waitcnt vmcnt(9)\n\t"
    "ds_read_b128 v[84:87], v13 offset:3072\n\t"
    "ds_read_b64_tr_b16 a[124:125], v5 offset:10240\n\t"
    "v_mfma_f32_16x16x32_bf16 v[32:35], a[4:7], v[60:63], v[32:35]\n\t"
    "s_waitcnt lgkmcnt(13)\n\t"
    "ds_read_b128 v[88:91], v12 offset:4096\n\t"
    "ds_read_b64_tr_b16 a[126:127], v5 offset:10496\n\t"
    "v_mfma_f32_16x16x32_bf16 v[32:35], a[8:11], v[64:67], v[32:35]\n\t"
    "s_waitcnt lgkmcnt(14)\n\t"
    "buffer_store_dwordx4 v[124:127], v16, s[24:27], s64 offen nt\n\t"
    "v_mfma_f32_16x16x32_bf16 v[32:35], a[12:15], v[68:71], v[32:35]\n\t"
    "s_waitcnt lgkmcnt(13)\n\t"
    "ds_read_b128 v[92:95], v13 offset:4096\n\t"
    "v_cvt_pk_bf16_f32 v20, v24, v25\n\t"
    "v_mfma_f32_16x16x32_bf16 v[32:35], a[16:19], v[72:75], v[32:35]\n\t"
    "ds_read_b128 v[96:99], v12 offset:5120\n\t"
    "v_cvt_pk_bf16_f32 v21, v26, v27\n\t"
    "v_mfma_f32_16x16x32_bf16 v[32:35], a[20:23], v[76:79], v[32:35]\n\t"
    "s_waitcnt lgkmcnt(14)\n\t"
    "v_cvt_pk_bf16_f32 v22, v28, v29\n\t"
    "v_cvt_pk_bf16_f32 v23, v30, v31\n\t"
    "v_mfma_f32_16x16x32_bf16 v[32:35], a[24:27], v[80:83], v[32:35]\n\t"
    "ds_read_b128 v[100:103], v13 offset:5120\n\t"
    "v_cndmask_b32_dpp v128, v22, v20, vcc quad_perm:[1,0,3,2] row_mask:0xf bank_mask:0xf\n\t"
    "v_mfma_f32_16x16x32_bf16 v[36:39], a[56:59], v[56:59], 0\n\t"
    "s_waitcnt lgkmcnt(14)\n\t"
    "ds_read_b128 v[104:107], v12 offset:6144\n\t"
    "v_cndmask_b32_dpp v129, v23, v21, vcc quad_perm:[1,0,3,2] row_mask:0xf bank_mask:0xf\n\t"
    "v_mfma_f32_16x16x32_bf16 v[36:39], a[60:63], v[60:63], v[36:39]\n\t"
    "s_add_u32 m0, s43, 6144\n\t"
    "s_mov_b64 vcc, s[34:35]\n\t"
    "v_cndmask_b32_dpp v130, v20, v22, vcc quad_perm:[1,0,3,2] row_mask:0xf bank_mask:0xf\n\t"
    "v_mfma_f32_16x16x32_bf16 v[36:39], a[64:67], v[64:67], v[36:39]\n\t"
    "s_waitcnt lgkmcnt(14)\n\t"
    "ds_read_b128 v[108:111], v13 offset:6144\n\t"
    "v_cndmask_b32_dpp v131, v21, v23, vcc quad_perm:[1,0,3,2] row_mask:0xf bank_mask:0xf\n\t"
    "v_mfma_f32_16x16x32_bf16 v[36:39], a[68:71], v[68:71], v[36:39]\n\t"
    "s_waitcnt lgkmcnt(13)\n\t"
    "buffer_load_dwordx4 v2, s[20:23], s78 offen  lds\n\t"
    "v_mfma_f32_16x16x32_bf16 v[36:39], a[72:75], v[72:75], v[36:39]\n\t"
    "v_permlane16_swap_b32 v128, v130\n\t"
    "v_permlane16_swap_b32 v129, v131\n\t"
    "v_mfma_f32_16x16x32_bf16 v[36:39], a[76:79], v[76:79], v[36:39]\n\t"
    "s_add_u32 m0, s43, 7168\n\t"
    "ds_read_b64_tr_b16 a[180:181], v6 offset:10240\n\t"
    "ds_read_b64_tr_b16 a[182:183], v6 offset:10496\n\t"
    "v_mfma_f32_16x16x32_bf16 v[36:39], a[80:83], v[80:83], v[36:39]\n\t"
    "s_waitcnt lgkmcnt(8)\n\t"
    "buffer_load_dwordx4 v2, s[20:23], s79 offen  lds\n\t"
    "v_mfma_f32_16x16x32_bf16 v[32:35], a[28:31], v[84:87], v[32:35]\n\t"
    "s_waitcnt vmcnt(10)\n\t"
    "ds_read_b128 v[56:59], v12 offset:28672\n\t"
    "ds_read_b64_tr_b16 a[128:129], v5 offset:0\n\t"
    "v_mfma_f32_16x16x32_bf16 v[32:35], a[32:35], v[88:91], v[32:35]\n\t"
    "s_waitcnt lgkmcnt(7)\n\t"
    "ds_read_b128 v[60:63], v13 offset:28672\n\t"
    "ds_read_b64_tr_b16 a[130:131], v5 offset:256\n\t"
    "v_mfma_f32_16x16x32_bf16 v[32:35], a[36:39], v[92:95], v[32:35]\n\t"
    "ds_read_b64_tr_b16 a[184:185], v6 offset:0\n\t"
    "ds_read_b64_tr_b16 a[186:187], v6 offset:256\n\t"
    "v_mfma_f32_16x16x32_bf16 v[32:35], a[40:43], v[96:99], v[32:35]\n\t"
    "s_waitcnt lgkmcnt(9)\n\t"
    "ds_read_b128 v[64:67], v12 offset:29696\n\t"
    "s_add_u32 m0, s43, 8192\n\t"
    "v_mfma_f32_16x16x32_bf16 v[32:35], a[44:47], v[100:103], v[32:35]\n\t"
    "s_waitcnt vmcnt(8)\n\t"
    "ds_read_b128 v[68:71], v13 offset:29696\n\t"
    "ds_read_b64_tr_b16 a[132:133], v5 offset:2048\n\t"
    "v_mfma_f32_16x16x32_bf16 v[32:35], a[48:51], v[104:107], v[32:35]\n\t"
    "s_waitcnt lgkmcnt(11)\n\t"
    "buffer_load_dwordx4 v2, s[20:23], s80 offen  lds\n\t"
    "v_mfma_f32_16x16x32_bf16 v[32:35], a[52:55], v[108:111], v[32:35]\n\t"
    "s_add_u32 m0, s43, 9216\n\t"
    "ds_read_b128 v[72:75], v12 offset:30720\n\t"
    "ds_read_b64_tr_b16 a[134:135], v5 offset:2304\n\t"
    "v_mfma_f32_16x16x32_bf16 v[36:39], a[84:87], v[84:87], v[36:39]\n\t"
    "ds_read_b128 v[76:79], v13 offset:30720\n\t"
    "ds_read_b64_tr_b16 a[188:189], v6 offset:2048\n\t"
    "v_mfma_f32_16x16x32_bf16 v[36:39], a[88:91], v[88:91], v[36:39]\n\t"
    "s_waitcnt lgkmcnt(14)\n\t"
    "buffer_load_dwordx4 v2, s[20:23], s81 offen  lds\n\t"
    "v_mfma_f32_16x16x32_bf16 v[36:39], a[92:95], v[92:95], v[36:39]\n\t"
    "ds_read_b128 v[80:83], v12 offset:31744\n\t"
    "v_mfma_f32_16x16x32_bf16 v[36:39], a[96:99], v[96:99], v[36:39]\n\t"
    "s_waitcnt lgkmcnt(14)\n\t"
    "ds_read_b64_tr_b16 a[190:191], v6 offset:2304\n\t"
    "v_mfma_f32_16x16x32_bf16 v[36:39], a[100:103], v[100:103], v[36:39]\n\t"
    "s_add_u32 m0, s43, 10240\n\t"
    "s_waitcnt vmcnt(8) lgkmcnt(14)\n\t"
    "ds_read_b64_tr_b16 a[136:137], v5 offset:4096\n\t"
    "v_mfma_f32_16x16x32_bf16 v[36:39], a[104:107], v[104:107], v[36:39]\n\t"
    "s_waitcnt lgkmcnt(14)\n\t"
    "buffer_load_dwordx4 v2, s[20:23], s82 offen  lds\n\t"
    "v_mfma_f32_16x16x32_bf16 v[36:39], a[108:111], v[108:111], v[36:39]\n\t"
    "s_waitcnt lgkmcnt(13)\n\t"
    "ds_read_b64_tr_b16 a[138:139], v5 offset:4352\n\t"
    "ds_read_b64_tr_b16 a[192:193], v6 offset:4096\n\t"
    "v_mfma_f32_16x16x32_bf16 v[40:43], a[0:3], v[56:59], 0\n\t"
    "s_waitcnt lgkmcnt(14)\n\t"
    "ds_read_b128 v[84:87], v14 offset:28672\n\t"
    "v_mfma_f32_16x16x32_bf16 v[40:43], a[4:7], v[60:63], v[40:43]\n\t"
    "s_waitcnt lgkmcnt(11)\n\t"
    "s_add_u32 m0, s43, 11264\n\t"
    "ds_read_b64_tr_b16 a[194:195], v6 offset:4352\n\t"
    "v_mfma_f32_16x16x32_bf16 v[40:43], a[8:11], v[64:67], v[40:43]\n\t"
    "ds_read_b128 v[88:91], v15 offset:28672\n\t"
    "v_cvt_pk_bf16_f32 v20, v32, v33\n\t"
    "v_mfma_f32_16x16x32_bf16 v[40:43], a[12:15], v[68:71], v[40:43]\n\t"
    "s_waitcnt lgkmcnt(9)\n\t"
    "buffer_load_dwordx4 v2, s[20:23], s83 offen  lds\n\t"
    "v_mfma_f32_16x16x32_bf16 v[40:43], a[16:19], v[72:75], v[40:43]\n\t"
    "v_cvt_pk_bf16_f32 v21, v34, v35\n\t"
    "v_cvt_pk_bf16_f32 v22, v36, v37\n\t"
    "v_mfma_f32_16x16x32_bf16 v[40:43], a[20:23], v[76:79], v[40:43]\n\t"
    "s_waitcnt lgkmcnt(7)\n\t"
    "ds_read_b128 v[92:95], v14 offset:29696\n\t"
    "v_cvt_pk_bf16_f32 v23, v38, v39\n\t"
    "v_mfma_f32_16x16x32_bf16 v[40:43], a[24:27], v[80:83], v[40:43]\n\t"
    "s_add_u32 m0, s43, 0\n\t"
    "v_cndmask_b32_dpp v134, v20, v22, vcc quad_perm:[1,0,3,2] row_mask:0xf bank_mask:0xf\n\t"
    "v_cndmask_b32_dpp v135, v21, v23, vcc quad_perm:[1,0,3,2] row_mask:0xf bank_mask:0xf\n\t"
    "v_mfma_f32_16x16x32_bf16 v[44:47], a[56:59], v[56:59], 0\n\t"
    "s_mov_b64 vcc, s[46:47]\n\t"
    "buffer_load_dwordx4 v2, s[20:23], s84 offen  lds\n\t"
    "v_mfma_f32_16x16x32_bf16 v[44:47], a[60:63], v[60:63], v[44:47]\n\t"
    "ds_read_b128 v[96:99], v15 offset:29696\n\t"
    "v_cndmask_b32_dpp v132, v22, v20, vcc quad_perm:[1,0,3,2] row_mask:0xf bank_mask:0xf\n\t"
    "v_mfma_f32_16x16x32_bf16 v[44:47], a[64:67], v[64:67], v[44:47]\n\t"
    "s_add_u32 m0, s43, 1024\n\t"
    "v_cndmask_b32_dpp v133, v23, v21, vcc quad_perm:[1,0,3,2] row_mask:0xf bank_mask:0xf\n\t"
    "v_permlane16_swap_b32 v132, v134\n\t"
    "v_mfma_f32_16x16x32_bf16 v[44:47], a[68:71], v[68:71], v[44:47]\n\t"
    "buffer_load_dwordx4 v2, s[20:23], s85 offen  lds\n\t"
    "v_mfma_f32_16x16x32_bf16 v[44:47], a[72:75], v[72:75], v[44:47]\n\t"
    "ds_read_b128 v[100:103], v14 offset:30720\n\t"
    "v_permlane16_swap_b32 v133, v135\n\t"
    "v_mfma_f32_16x16x32_bf16 v[44:47], a[76:79], v[76:79], v[44:47]\n\t"
    "v_mfma_f32_16x16x32_bf16 v[44:47], a[80:83], v[80:83], v[44:47]\n\t"
    "v_mfma_f32_16x16x32_bf16 v[48:51], a[112:115], v[56:59], 0\n\t"
    "s_add_u32 m0, s43, 2048\n\t"
    "ds_read_b128 v[104:107], v15 offset:30720\n\t"
    "v_mfma_f32_16x16x32_bf16 v[48:51], a[116:119], v[60:63], v[48:51]\n\t"
    "s_waitcnt lgkmcnt(10)\n\t"
    "buffer_load_dwordx4 v2, s[20:23], s86 offen  lds\n\t"
    "v_mfma_f32_16x16x32_bf16 v[48:51], a[120:123], v[64:67], v[48:51]\n\t"
    "v_mfma_f32_16x16x32_bf16 v[48:51], a[124:127], v[68:71], v[48:51]\n\t"
    "s_add_u32 m0, s43, 3072\n\t"
    "ds_read_b128 v[108:111], v14 offset:31744\n\t"
    "v_mfma_f32_16x16x32_bf16 v[48:51], a[128:131], v[72:75], v[48:51]\n\t"
    "s_waitcnt vmcnt(7)\n\t"
    "buffer_load_dwordx4 v2, s[20:23], s87 offen  lds\n\t"
    "v_mfma_f32_16x16x32_bf16 v[48:51], a[132:135], v[76:79], v[48:51]\n\t"
    "s_waitcnt lgkmcnt(9)\n\t"
    "ds_read_b64_tr_b16 a[140:141], v5 offset:6144\n\t"
    "ds_read_b64_tr_b16 a[142:143], v5 offset:6400\n\t"
    "v_mfma_f32_16x16x32_bf16 v[48:51], a[136:139], v[80:83], v[48:51]\n\t"
    "ds_read_b64_tr_b16 a[196:197], v6 offset:6144\n\t"
    "ds_read_b64_tr_b16 a[198:199], v6 offset:6400\n\t"
    "v_mfma_f32_16x16x32_bf16 v[52:55], a[168:171], v[56:59], 0\n\t"
    "v_mfma_f32_16x16x32_bf16 v[52:55], a[172:175], v[60:63], v[52:55]\n\t"
    "v_mfma_f32_16x16x32_bf16 v[52:55], a[176:179], v[64:67], v[52:55]\n\t"
    "s_add_u32 m0, s43, 4096\n\t"
    "v_mfma_f32_16x16x32_bf16 v[52:55], a[180:183], v[68:71], v[52:55]\n\t"
    "s_waitcnt lgkmcnt(10)\n\t"
    "buffer_load_dwordx4 v2, s[20:23], s88 offen  lds\n\t"
    "v_mfma_f32_16x16x32_bf16 v[52:55], a[184:187], v[72:75], v[52:55]\n\t"
    "s_waitcnt vmcnt(7)\n\t"
    "ds_read_b64_tr_b16 a[144:145], v5 offset:8192\n\t"
    "ds_read_b64_tr_b16 a[146:147], v5 offset:8448\n\t"
    "v_mfma_f32_16x16x32_bf16 v[52:55], a[188:191], v[76:79], v[52:55]\n\t"
    "s_add_u32 m0, s43, 5120\n\t"
    "ds_read_b64_tr_b16 a[200:201], v6 offset:8192\n\t"
    "ds_read_b64_tr_b16 a[202:203], v6 offset:8448\n\t"
    "v_mfma_f32_16x16x32_bf16 v[52:55], a[192:195], v[80:83], v[52:55]\n\t"
    "s_waitcnt lgkmcnt(13)\n\t"
    "buffer_load_dwordx4 v2, s[20:23], s89 offen  lds\n\t"
    "v_mfma_f32_16x16x32_bf16 v[24:27], a[0:3], v[84:87], 0\n\t"
    "ds_read_b128 v[56:59], v13 offset:31744\n\t"
    "v_mfma_f32_16x16x32_bf16 v[24:27], a[4:7], v[88:91], v[24:27]\n\t"
    "s_waitcnt lgkmcnt(12)\n\t"
    "v_mfma_f32_16x16x32_bf16 v[24:27], a[8:11], v[92:95], v[24:27]\n\t"
    "ds_read_b128 v[60:63], v12 offset:32768\n\t"
    "v_mfma_f32_16x16x32_bf16 v[24:27], a[12:15], v[96:99], v[24:27]\n\t"
    "s_waitcnt lgkmcnt(11)\n\t"
    "s_add_u32 m0, s43, 6144\n\t"
    "v_mfma_f32_16x16x32_bf16 v[24:27], a[16:19], v[100:103], v[24:27]\n\t"
    "s_waitcnt lgkmcnt(6)\n\t"
    "buffer_load_dwordx4 v2, s[20:23], s90 offen  lds\n\t"
    "v_mfma_f32_16x16x32_bf16 v[24:27], a[20:23], v[104:107], v[24:27]\n\t"
    "ds_read_b128 v[64:67], v13 offset:32768\n\t"
    "v_mfma_f32_16x16x32_bf16 v[24:27], a[24:27], v[108:111], v[24:27]\n\t"
    "s_add_u32 m0, s43, 7168\n\t"
    "s_waitcnt vmcnt(7)\n\t"
    "ds_read_b64_tr_b16 a[148:149], v5 offset:10240\n\t"
    "v_mfma_f32_16x16x32_bf16 v[28:31], a[56:59], v[84:87], 0\n\t"
    "buffer_load_dwordx4 v2, s[20:23], s91 offen  lds\n\t"
    "v_mfma_f32_16x16x32_bf16 v[28:31], a[60:63], v[88:91], v[28:31]\n\t"
    "ds_read_b128 v[68:71], v12 offset:33792\n\t"
    "ds_read_b64_tr_b16 a[150:151], v5 offset:10496\n\t"
    "v_mfma_f32_16x16x32_bf16 v[28:31], a[64:67], v[92:95], v[28:31]\n\t"
    "ds_read_b64_tr_b16 a[204:205], v6 offset:10240\n\t"
    "ds_read_b64_tr_b16 a[206:207], v6 offset:10496\n\t"
    "v_mfma_f32_16x16x32_bf16 v[28:31], a[68:71], v[96:99], v[28:31]\n\t"
    "v_mfma_f32_16x16x32_bf16 v[28:31], a[72:75], v[100:103], v[28:31]\n\t"
    "ds_read_b128 v[72:75], v13 offset:33792\n\t"
    "v_add_u32 v2, 256, v2\n\t"
    "v_mfma_f32_16x16x32_bf16 v[28:31], a[76:79], v[104:107], v[28:31]\n\t"
    "s_add_u32 m0, s43, 8192\n\t"
    "v_mfma_f32_16x16x32_bf16 v[28:31], a[80:83], v[108:111], v[28:31]\n\t"
    "s_waitcnt lgkmcnt(9)\n\t"
    "buffer_load_dwordx4 v2, s[20:23], s64 offen  lds\n\t"
    "v_mfma_f32_16x16x32_bf16 v[32:35], a[112:115], v[84:87], 0\n\t"
    "s_waitcnt vmcnt(7)\n\t"
    "ds_read_b128 v[76:79], v12 offset:34816\n\t"
    "ds_read_b64_tr_b16 a[152:153], v5 offset:0\n\t"
    "v_mfma_f32_16x16x32_bf16 v[32:35], a[116:119], v[88:91], v[32:35]\n\t"
    "s_add_u32 m0, s43, 9216\n\t"
    "ds_read_b64_tr_b16 a[154:155], v5 offset:256\n\t"
    "ds_read_b64_tr_b16 a[208:209], v6 offset:0\n\t"
    "v_mfma_f32_16x16x32_bf16 v[32:35], a[120:123], v[92:95], v[32:35]\n\t"
    "buffer_load_dwordx4 v2, s[20:23], s65 offen  lds\n\t"
    "v_mfma_f32_16x16x32_bf16 v[32:35], a[124:127], v[96:99], v[32:35]\n\t"
    "ds_read_b128 v[80:83], v13 offset:34816\n\t"
    "ds_read_b64_tr_b16 a[210:211], v6 offset:256\n\t"
    "v_mfma_f32_16x16x32_bf16 v[32:35], a[128:131], v[100:103], v[32:35]\n\t"
    "v_mfma_f32_16x16x32_bf16 v[32:35], a[132:135], v[104:107], v[32:35]\n\t"
    "v_mfma_f32_16x16x32_bf16 v[32:35], a[136:139], v[108:111], v[32:35]\n\t"
    "s_add_u32 m0, s43, 10240\n\t"
    "v_mfma_f32_16x16x32_bf16 v[36:39], a[168:171], v[84:87], 0\n\t"
    "s_waitcnt lgkmcnt(7)\n\t"
    "buffer_load_dwordx4 v2, s[20:23], s66 offen  lds\n\t"
    "v_mfma_f32_16x16x32_bf16 v[36:39], a[172:175], v[88:91], v[36:39]\n\t"
    "v_mfma_f32_16x16x32_bf16 v[36:39], a[176:179], v[92:95], v[36:39]\n\t"
    "s_add_u32 m0, s43, 11264\n\t"
    "s_waitcnt vmcnt(7)\n\t"
    "ds_read_b64_tr_b16 a[156:157], v5 offset:2048\n\t"
    "v_mfma_f32_16x16x32_bf16 v[36:39], a[180:183], v[96:99], v[36:39]\n\t"
    "buffer_load_dwordx4 v2, s[20:23], s67 offen  lds\n\t"
    "v_mfma_f32_16x16x32_bf16 v[36:39], a[184:187], v[100:103], v[36:39]\n\t"
    "ds_read_b64_tr_b16 a[158:159], v5 offset:2304\n\t"
    "ds_read_b64_tr_b16 a[212:213], v6 offset:2048\n\t"
    "v_mfma_f32_16x16x32_bf16 v[36:39], a[188:191], v[104:107], v[36:39]\n\t"
    "ds_read_b64_tr_b16 a[214:215], v6 offset:2304\n\t"
    "v_mfma_f32_16x16x32_bf16 v[36:39], a[192:195], v[108:111], v[36:39]\n\t"
    "v_mfma_f32_16x16x32_bf16 v[40:43], a[28:31], v[56:59], v[40:43]\n\t"
    "s_add_u32 m0, s43, 0\n\t"
    "ds_read_b128 v[84:87], v15 offset:31744\n\t"
    "v_mfma_f32_16x16x32_bf16 v[40:43], a[32:35], v[60:63], v[40:43]\n\t"
    "s_waitcnt lgkmcnt(5)\n\t"
    "buffer_load_dwordx4 v2, s[20:23], s68 offen  lds\n\t"
    "v_mfma_f32_16x16x32_bf16 v[40:43], a[36:39], v[64:67], v[40:43]\n\t"
    "s_waitcnt vmcnt(7)\n\t"
    "ds_read_b128 v[88:91], v14 offset:32768\n\t"
    "ds_read_b64_tr_b16 a[160:161], v5 offset:4096\n\t"
    "v_mfma_f32_16x16x32_bf16 v[40:43], a[40:43], v[68:71], v[40:43]\n\t"
    "s_add_u32 m0, s43, 1024\n\t"
    "ds_read_b64_tr_b16 a[162:163], v5 offset:4352\n\t"
    "ds_read_b64_tr_b16 a[216:217], v6 offset:4096\n\t"
    "v_mfma_f32_16x16x32_bf16 v[40:43], a[44:47], v[72:75], v[40:43]\n\t"
    "buffer_load_dwordx4 v2, s[20:23], s69 offen  lds\n\t"
    "v_mfma_f32_16x16x32_bf16 v[40:43], a[48:51], v[76:79], v[40:43]\n\t"
    "ds_read_b128 v[92:95], v15 offset:32768\n\t"
    "ds_read_b64_tr_b16 a[218:219], v6 offset:4352\n\t"
    "v_mfma_f32_16x16x32_bf16 v[40:43], a[52:55], v[80:83], v[40:43]\n\t"
    "s_waitcnt vmcnt(6)\n\t"
    "ds_read_b64_tr_b16 a[164:165], v5 offset:6144\n\t"
    "ds_read_b64_tr_b16 a[166:167], v5 offset:6400\n\t"
    "v_mfma_f32_16x16x32_bf16 v[44:47], a[84:87], v[56:59], v[44:47]\n\t"
    "ds_read_b64_tr_b16 a[220:221], v6 offset:6144\n\t"
    "ds_read_b64_tr_b16 a[222:223], v6 offset:6400\n\t"
    "v_mfma_f32_16x16x32_bf16 v[44:47], a[88:91], v[60:63], v[44:47]\n\t"
    "s_waitcnt lgkmcnt(14)\n\t"
    "ds_read_b128 v[96:99], v14 offset:33792\n\t"
    "v_mfma_f32_16x16x32_bf16 v[44:47], a[92:95], v[64:67], v[44:47]\n\t"
    "s_add_u32 m0, s43, 2048\n\t"
    "v_mfma_f32_16x16x32_bf16 v[44:47], a[96:99], v[68:71], v[44:47]\n\t"
    "s_waitcnt lgkmcnt(12)\n\t"
    "buffer_load_dwordx4 v2, s[20:23], s70 offen  lds\n\t"
    "v_mfma_f32_16x16x32_bf16 v[44:47], a[100:103], v[72:75], v[44:47]\n\t"
    "ds_read_b128 v[100:103], v15 offset:33792\n\t"
    "v_mfma_f32_16x16x32_bf16 v[44:47], a[104:107], v[76:79], v[44:47]\n\t"
    "s_add_u32 m0, s43, 3072\n\t"
    "v_mfma_f32_16x16x32_bf16 v[44:47], a[108:111], v[80:83], v[44:47]\n\t"
    "buffer_load_dwordx4 v2, s[20:23], s71 offen  lds\n\t"
    "v_mfma_f32_16x16x32_bf16 v[48:51], a[140:143], v[56:59], v[48:51]\n\t"
    "ds_read_b128 v[104:107], v14 offset:34816\n\t"
    "v_mfma_f32_16x16x32_bf16 v[48:51], a[144:147], v[60:63], v[48:51]\n\t"
    "v_mfma_f32_16x16x32_bf16 v[48:51], a[148:151], v[64:67], v[48:51]\n\t"
    "v_cvt_pk_bf16_f32 v20, v40, v41\n\t"
    "v_cvt_pk_bf16_f32 v21, v42, v43\n\t"
    "v_mfma_f32_16x16x32_bf16 v[48:51], a[152:155], v[68:71], v[48:51]\n\t"
    "s_add_u32 m0, s43, 4096\n\t"
    "ds_read_b128 v[108:111], v15 offset:34816\n\t"
    "v_cvt_pk_bf16_f32 v22, v44, v45\n\t"
    "v_mfma_f32_16x16x32_bf16 v[48:51], a[156:159], v[72:75], v[48:51]\n\t"
    "s_waitcnt lgkmcnt(11)\n\t"
    "v_cvt_pk_bf16_f32 v23, v46, v47\n\t"
    "v_cndmask_b32_dpp v248, v22, v20, vcc quad_perm:[1,0,3,2] row_mask:0xf bank_mask:0xf\n\t"
    "v_mfma_f32_16x16x32_bf16 v[48:51], a[160:163], v[76:79], v[48:51]\n\t"
    "s_waitcnt lgkmcnt(6)\n\t"
    "buffer_load_dwordx4 v2, s[20:23], s72 offen  lds\n\t"
    "v_mfma_f32_16x16x32_bf16 v[48:51], a[164:167], v[80:83], v[48:51]\n\t"
    "s_add_u32 m0, s43, 5120\n\t"
    "v_cndmask_b32_dpp v249, v23, v21, vcc quad_perm:[1,0,3,2] row_mask:0xf bank_mask:0xf\n\t"
    "s_mov_b64 vcc, s[34:35]\n\t"
    "v_mfma_f32_16x16x32_bf16 v[52:55], a[196:199], v[56:59], v[52:55]\n\t"
    "s_waitcnt vmcnt(7)\n\t"
    "buffer_load_dwordx4 v2, s[20:23], s73 offen  lds\n\t"
    "v_mfma_f32_16x16x32_bf16 v[52:55], a[200:203], v[60:63], v[52:55]\n\t"
    "v_cndmask_b32_dpp v250, v20, v22, vcc quad_perm:[1,0,3,2] row_mask:0xf bank_mask:0xf\n\t"
    "v_cndmask_b32_dpp v251, v21, v23, vcc quad_perm:[1,0,3,2] row_mask:0xf bank_mask:0xf\n\t"
    "v_mfma_f32_16x16x32_bf16 v[52:55], a[204:207], v[64:67], v[52:55]\n\t"
    "v_permlane16_swap_b32 v248, v250\n\t"
    "v_permlane16_swap_b32 v249, v251\n\t"
    "v_mfma_f32_16x16x32_bf16 v[52:55], a[208:211], v[68:71], v[52:55]\n\t"
    "ds_read_b64_tr_b16 v[136:137], v5 offset:8192\n\t"
    "ds_read_b64_tr_b16 v[138:139], v5 offset:8448\n\t"
    "v_mfma_f32_16x16x32_bf16 v[52:55], a[212:215], v[72:75], v[52:55]\n\t"
    "s_add_u32 m0, s43, 6144\n\t"
    "ds_read_b64_tr_b16 v[192:193], v6 offset:8192\n\t"
    "ds_read_b64_tr_b16 v[194:195], v6 offset:8448\n\t"
    "v_mfma_f32_16x16x32_bf16 v[52:55], a[216:219], v[76:79], v[52:55]\n\t"
    "s_waitcnt lgkmcnt(8)\n\t"
    "buffer_load_dwordx4 v2, s[20:23], s74 offen  lds\n\t"
    "v_mfma_f32_16x16x32_bf16 v[52:55], a[220:223], v[80:83], v[52:55]\n\t"
    "v_mfma_f32_16x16x32_bf16 v[24:27], a[28:31], v[84:87], v[24:27]\n\t"
    "s_waitcnt vmcnt(7)\n\t"
    "ds_read_b128 v[56:59], v10 offset:0\n\t"
    "ds_read_b64_tr_b16 v[140:141], v5 offset:10240\n\t"
    "v_mfma_f32_16x16x32_bf16 v[24:27], a[32:35], v[88:91], v[24:27]\n\t"
    "s_waitcnt lgkmcnt(9)\n\t"
    "s_add_u32 m0, s43, 7168\n\t"
    "ds_read_b64_tr_b16 v[142:143], v5 offset:10496\n\t"
    "v_mfma_f32_16x16x32_bf16 v[24:27], a[36:39], v[92:95], v[24:27]\n\t"
    "ds_read_b128 v[60:63], v11 offset:0\n\t"
    "v_cvt_pk_bf16_f32 v20, v48, v49\n\t"
    "v_mfma_f32_16x16x32_bf16 v[24:27], a[40:43], v[96:99], v[24:27]\n\t"
    "s_waitcnt lgkmcnt(9)\n\t"
    "buffer_load_dwordx4 v2, s[20:23], s75 offen  lds\n\t"
    "v_mfma_f32_16x16x32_bf16 v[24:27], a[44:47], v[100:103], v[24:27]\n\t"
    "v_cvt_pk_bf16_f32 v21, v50, v51\n\t"
    "v_cvt_pk_bf16_f32 v22, v52, v53\n\t"
    "v_mfma_f32_16x16x32_bf16 v[24:27], a[48:51], v[104:107], v[24:27]\n\t"
    "s_waitcnt lgkmcnt(8)\n\t"
    "ds_read_b128 v[64:67], v10 offset:1024\n\t"
    "v_cvt_pk_bf16_f32 v23, v54, v55\n\t"
    "v_mfma_f32_16x16x32_bf16 v[24:27], a[52:55], v[108:111], v[24:27]\n\t"
    "s_add_u32 m0, s43, 8192\n\t"
    "v_cndmask_b32_dpp v114, v20, v22, vcc quad_perm:[1,0,3,2] row_mask:0xf bank_mask:0xf\n\t"
    "v_cndmask_b32_dpp v115, v21, v23, vcc quad_perm:[1,0,3,2] row_mask:0xf bank_mask:0xf\n\t"
    "v_mfma_f32_16x16x32_bf16 v[28:31], a[84:87], v[84:87], v[28:31]\n\t"
    "s_waitcnt lgkmcnt(5)\n\t"
    "buffer_load_dwordx4 v2, s[20:23], s76 offen  lds\n\t"
    "v_mfma_f32_16x16x32_bf16 v[28:31], a[88:91], v[88:91], v[28:31]\n\t"
    "s_mov_b64 vcc, s[46:47]\n\t"
    "ds_read_b128 v[68:71], v11 offset:1024\n\t"
    "v_cndmask_b32_dpp v112, v22, v20, vcc quad_perm:[1,0,3,2] row_mask:0xf bank_mask:0xf\n\t"
    "v_mfma_f32_16x16x32_bf16 v[28:31], a[92:95], v[92:95], v[28:31]\n\t"
    "s_add_u32 m0, s43, 9216\n\t"
    "v_cndmask_b32_dpp v113, v23, v21, vcc quad_perm:[1,0,3,2] row_mask:0xf bank_mask:0xf\n\t"
    "v_permlane16_swap_b32 v112, v114\n\t"
    "v_mfma_f32_16x16x32_bf16 v[28:31], a[96:99], v[96:99], v[28:31]\n\t"
    "buffer_load_dwordx4 v2, s[20:23], s77 offen  lds\n\t"
    "v_mfma_f32_16x16x32_bf16 v[28:31], a[100:103], v[100:103], v[28:31]\n\t"
    "ds_read_b128 v[72:75], v10 offset:2048\n\t"
    "v_permlane16_swap_b32 v113, v115\n\t"
    "v_mfma_f32_16x16x32_bf16 v[28:31], a[104:107], v[104:107], v[28:31]\n\t"
    "ds_read_b64_tr_b16 v[196:197], v6 offset:10240\n\t"
    "ds_read_b64_tr_b16 v[198:199], v6 offset:10496\n\t"
    "v_mfma_f32_16x16x32_bf16 v[28:31], a[108:111], v[108:111], v[28:31]\n\t"
    "s_waitcnt vmcnt(8)\n\t"
    "ds_read_b64_tr_b16 v[144:145], v5 offset:0\n\t"
    "ds_read_b64_tr_b16 v[146:147], v5 offset:256\n\t"
    "v_mfma_f32_16x16x32_bf16 v[32:35], a[140:143], v[84:87], v[32:35]\n\t"
    "ds_read_b128 v[76:79], v11 offset:2048\n\t"
    "ds_read_b64_tr_b16 v[200:201], v6 offset:0\n\t"
    "v_mfma_f32_16x16x32_bf16 v[32:35], a[144:147], v[88:91], v[32:35]\n\t"
    "ds_read_b64_tr_b16 v[202:203], v6 offset:256\n\t"
    "v_mfma_f32_16x16x32_bf16 v[32:35], a[148:151], v[92:95], v[32:35]\n\t"
    "buffer_store_dwordx4 v[128:131], v16, s[24:27], s66 offen nt\n\t"
    "v_mfma_f32_16x16x32_bf16 v[32:35], a[152:155], v[96:99], v[32:35]\n\t"
    "s_waitcnt vmcnt(7)\n\t"
    "ds_read_b128 v[80:83], v10 offset:3072\n\t"
    "v_cvt_pk_bf16_f32 v20, v24, v25\n\t"
    "v_mfma_f32_16x16x32_bf16 v[32:35], a[156:159], v[100:103], v[32:35]\n\t"
    "s_waitcnt lgkmcnt(14)\n\t"
    "v_cvt_pk_bf16_f32 v21, v26, v27\n\t"
    "v_cvt_pk_bf16_f32 v22, v28, v29\n\t"
    "v_mfma_f32_16x16x32_bf16 v[32:35], a[160:163], v[104:107], v[32:35]\n\t"
    "v_cvt_pk_bf16_f32 v23, v30, v31\n\t"
    "v_cndmask_b32_dpp v116, v22, v20, vcc quad_perm:[1,0,3,2] row_mask:0xf bank_mask:0xf\n\t"
    "v_mfma_f32_16x16x32_bf16 v[32:35], a[164:167], v[108:111], v[32:35]\n\t"
    "v_cndmask_b32_dpp v117, v23, v21, vcc quad_perm:[1,0,3,2] row_mask:0xf bank_mask:0xf\n\t"
    "s_mov_b64 vcc, s[34:35]\n\t"
    "v_mfma_f32_16x16x32_bf16 v[36:39], a[196:199], v[84:87], v[36:39]\n\t"
    "v_cndmask_b32_dpp v118, v20, v22, vcc quad_perm:[1,0,3,2] row_mask:0xf bank_mask:0xf\n\t"
    "v_cndmask_b32_dpp v119, v21, v23, vcc quad_perm:[1,0,3,2] row_mask:0xf bank_mask:0xf\n\t"
    "v_mfma_f32_16x16x32_bf16 v[36:39], a[200:203], v[88:91], v[36:39]\n\t"
    "v_permlane16_swap_b32 v116, v118\n\t"
    "v_permlane16_swap_b32 v117, v119\n\t"
    "v_mfma_f32_16x16x32_bf16 v[36:39], a[204:207], v[92:95], v[36:39]\n\t"
    "s_add_u32 m0, s43, 10240\n\t"
    "ds_read_b64_tr_b16 v[148:149], v5 offset:2048\n\t"
    "v_mfma_f32_16x16x32_bf16 v[36:39], a[208:211], v[96:99], v[36:39]\n\t"
    "s_waitcnt lgkmcnt(7)\n\t"
    "buffer_load_dwordx4 v2, s[20:23], s78 offen  lds\n\t"
    "v_mfma_f32_16x16x32_bf16 v[36:39], a[212:215], v[100:103], v[36:39]\n\t"
    "ds_read_b64_tr_b16 v[150:151], v5 offset:2304\n\t"
    "ds_read_b64_tr_b16 v[204:205], v6 offset:2048\n\t"
    "v_mfma_f32_16x16x32_bf16 v[36:39], a[216:219], v[104:107], v[36:39]\n\t"
    "s_add_u32 m0, s43, 11264\n\t"
    "ds_read_b64_tr_b16 v[206:207], v6 offset:2304\n\t"
    "v_mfma_f32_16x16x32_bf16 v[36:39], a[220:223], v[108:111], v[36:39]\n\t"
    "s_waitcnt vmcnt(6)\n\t"
    "buffer_load_dwordx4 v2, s[20:23], s79 offen  lds\n\t"
    "v_mfma_f32_16x16x32_bf16 v[40:43], a[112:115], v[56:59], 0\n\t"
    "ds_read_b128 v[84:87], v10 offset:28672\n\t"
    "ds_read_b64_tr_b16 v[152:153], v5 offset:4096\n\t"
    "v_mfma_f32_16x16x32_bf16 v[40:43], a[116:119], v[60:63], v[40:43]\n\t"
    "ds_read_b64_tr_b16 v[154:155], v5 offset:4352\n\t"
    "ds_read_b64_tr_b16 v[208:209], v6 offset:4096\n\t"
    "v_mfma_f32_16x16x32_bf16 v[40:43], a[120:123], v[64:67], v[40:43]\n\t"
    "ds_read_b128 v[88:91], v11 offset:28672\n\t"
    "v_cvt_pk_bf16_f32 v20, v32, v33\n\t"
    "v_mfma_f32_16x16x32_bf16 v[40:43], a[124:127], v[68:71], v[40:43]\n\t"
    "s_waitcnt lgkmcnt(12)\n\t"
    "v_cvt_pk_bf16_f32 v21, v34, v35\n\t"
    "v_cvt_pk_bf16_f32 v22, v36, v37\n\t"
    "v_mfma_f32_16x16x32_bf16 v[40:43], a[128:131], v[72:75], v[40:43]\n\t"
    "s_add_u32 m0, s43, 0\n\t"
    "v_cvt_pk_bf16_f32 v23, v38, v39\n\t"
    "v_cndmask_b32_dpp v122, v20, v22, vcc quad_perm:[1,0,3,2] row_mask:0xf bank_mask:0xf\n\t"
    "v_mfma_f32_16x16x32_bf16 v[40:43], a[132:135], v[76:79], v[40:43]\n\t"
    "s_waitcnt lgkmcnt(9)\n\t"
    "ds_read_b128 v[92:95], v10 offset:29696\n\t"
    "v_cndmask_b32_dpp v123, v21, v23, vcc quad_perm:[1,0,3,2] row_mask:0xf bank_mask:0xf\n\t"
    "v_mfma_f32_16x16x32_bf16 v[40:43], a[136:139], v[80:83], v[40:43]\n\t"
    "s_mov_b64 vcc, s[46:47]\n\t"
    "buffer_load_dwordx4 v2, s[20:23], s80 offen  lds\n\t"
    "v_mfma_f32_16x16x32_bf16 v[44:47], a[168:171], v[56:59], 0\n\t"
    "s_add_u32 m0, s43, 1024\n\t"
    "v_cndmask_b32_dpp v120, v22, v20, vcc quad_perm:[1,0,3,2] row_mask:0xf bank_mask:0xf\n\t"
    "v_cndmask_b32_dpp v121, v23, v21, vcc quad_perm:[1,0,3,2] row_mask:0xf bank_mask:0xf\n\t"
    "v_mfma_f32_16x16x32_bf16 v[44:47], a[172:175], v[60:63], v[44:47]\n\t"
    "ds_read_b128 v[96:99], v11 offset:29696\n\t"
    "v_permlane16_swap_b32 v120, v122\n\t"
    "v_mfma_f32_16x16x32_bf16 v[44:47], a[176:179], v[64:67], v[44:47]\n\t"
    "buffer_load_dwordx4 v2, s[20:23], s81 offen  lds\n\t"
    "v_mfma_f32_16x16x32_bf16 v[44:47], a[180:183], v[68:71], v[44:47]\n\t"
    "v_permlane16_swap_b32 v121, v123\n\t"
    "ds_read_b64_tr_b16 v[210:211], v6 offset:4352\n\t"
    "v_mfma_f32_16x16x32_bf16 v[44:47], a[184:187], v[72:75], v[44:47]\n\t"
    "s_waitcnt vmcnt(7)\n\t"
    "ds_read_b128 v[100:103], v10 offset:30720\n\t"
    "ds_read_b64_tr_b16 v[156:157], v5 offset:6144\n\t"
    "v_mfma_f32_16x16x32_bf16 v[44:47], a[188:191], v[76:79], v[44:47]\n\t"
    "s_add_u32 m0, s43, 2048\n\t"
    "ds_read_b64_tr_b16 v[158:159], v5 offset:6400\n\t"
    "v_mfma_f32_16x16x32_bf16 v[44:47], a[192:195], v[80:83], v[44:47]\n\t"
    "s_waitcnt lgkmcnt(11)\n\t"
    "buffer_load_dwordx4 v2, s[20:23], s82 offen  lds\n\t"
    "v_mfma_f32_16x16x32_bf16 v[48:51], v[136:139], v[56:59], 0\n\t"
    "ds_read_b128 v[104:107], v11 offset:30720\n\t"
    "ds_read_b64_tr_b16 v[212:213], v6 offset:6144\n\t"
    "v_mfma_f32_16x16x32_bf16 v[48:51], v[140:143], v[60:63], v[48:51]\n\t"
    "s_add_u32 m0, s43, 3072\n\t"
    "ds_read_b64_tr_b16 v[214:215], v6 offset:6400\n\t"
    "v_mfma_f32_16x16x32_bf16 v[48:51], v[144:147], v[64:67], v[48:51]\n\t"
    "s_waitcnt vmcnt(6)\n\t"
    "buffer_load_dwordx4 v2, s[20:23], s83 offen  lds\n\t"
    "v_mfma_f32_16x16x32_bf16 v[48:51], v[148:151], v[68:71], v[48:51]\n\t"
    "s_waitcnt lgkmcnt(11)\n\t"
    "ds_read_b128 v[108:111], v10 offset:31744\n\t"
    "ds_read_b64_tr_b16 v[160:161], v5 offset:8192\n\t"
    "v_mfma_f32_16x16x32_bf16 v[48:51], v[152:155], v[72:75], v[48:51]\n\t"
    "s_waitcnt lgkmcnt(5)\n\t"
    "ds_read_b64_tr_b16 v[162:163], v5 offset:8448\n\t"
    "ds_read_b64_tr_b16 v[216:217], v6 offset:8192\n\t"
    "v_mfma_f32_16x16x32_bf16 v[48:51], v[156:159], v[76:79], v[48:51]\n\t"
    "s_waitcnt lgkmcnt(1)\n\t"
    "ds_read_b64_tr_b16 v[218:219], v6 offset:8448\n\t"
    "v_mfma_f32_16x16x32_bf16 v[48:51], v[160:163], v[80:83], v[48:51]\n\t"
    "s_add_u32 m0, s43, 4096\n\t"
    "v_mfma_f32_16x16x32_bf16 v[52:55], v[192:195], v[56:59], 0\n\t"
    "buffer_load_dwordx4 v2, s[20:23], s84 offen  lds\n\t"
    "v_mfma_f32_16x16x32_bf16 v[52:55], v[196:199], v[60:63], v[52:55]\n\t"
    "v_mfma_f32_16x16x32_bf16 v[52:55], v[200:203], v[64:67], v[52:55]\n\t"
    "v_mfma_f32_16x16x32_bf16 v[52:55], v[204:207], v[68:71], v[52:55]\n\t"
    "s_add_u32 m0, s43, 5120\n\t"
    "v_mfma_f32_16x16x32_bf16 v[52:55], v[208:211], v[72:75], v[52:55]\n\t"
    "buffer_load_dwordx4 v2, s[20:23], s85 offen  lds\n\t"
    "v_mfma_f32_16x16x32_bf16 v[52:55], v[212:215], v[76:79], v[52:55]\n\t"
    "s_waitcnt lgkmcnt(0)\n\t"
    "v_mfma_f32_16x16x32_bf16 v[52:55], v[216:219], v[80:83], v[52:55]\n\t"
    "v_mfma_f32_16x16x32_bf16 v[24:27], a[112:115], v[84:87], 0\n\t"
    "ds_read_b128 v[56:59], v11 offset:3072\n\t"
    "v_mfma_f32_16x16x32_bf16 v[24:27], a[116:119], v[88:91], v[24:27]\n\t"
    "s_add_u32 m0, s43, 6144\n\t"
    "v_mfma_f32_16x16x32_bf16 v[24:27], a[120:123], v[92:95], v[24:27]\n\t"
    "ds_read_b128 v[60:63], v10 offset:4096\n\t"
    "v_mfma_f32_16x16x32_bf16 v[24:27], a[124:127], v[96:99], v[24:27]\n\t"
    "s_waitcnt vmcnt(6)\n\t"
    "buffer_load_dwordx4 v2, s[20:23], s86 offen  lds\n\t"
    "v_mfma_f32_16x16x32_bf16 v[24:27], a[128:131], v[100:103], v[24:27]\n\t"
    "s_add_u32 m0, s43, 7168\n\t"
    "ds_read_b64_tr_b16 v[164:165], v5 offset:10240\n\t"
    "ds_read_b64_tr_b16 v[166:167], v5 offset:10496\n\t"
    "v_mfma_f32_16x16x32_bf16 v[24:27], a[132:135], v[104:107], v[24:27]\n\t"
    "ds_read_b128 v[64:67], v11 offset:4096\n\t"
    "ds_read_b64_tr_b16 v[220:221], v6 offset:10240\n\t"
    "v_mfma_f32_16x16x32_bf16 v[24:27], a[136:139], v[108:111], v[24:27]\n\t"
    "buffer_load_dwordx4 v2, s[20:23], s87 offen  lds\n\t"
    "v_mfma_f32_16x16x32_bf16 v[28:31], a[168:171], v[84:87], 0\n\t"
    "ds_read_b64_tr_b16 v[222:223], v6 offset:10496\n\t"
    "v_mfma_f32_16x16x32_bf16 v[28:31], a[172:175], v[88:91], v[28:31]\n\t"
    "s_waitcnt vmcnt(6)\n\t"
    "ds_read_b128 v[68:71], v10 offset:5120\n\t"
    "ds_read_b64_tr_b16 v[168:169], v5 offset:0\n\t"
    "v_mfma_f32_16x16x32_bf16 v[28:31], a[176:179], v[92:95], v[28:31]\n\t"
    "s_add_u32 m0, s43, 8192\n\t"
    "ds_read_b64_tr_b16 v[170:171], v5 offset:256\n\t"
    "ds_read_b64_tr_b16 v[224:225], v6 offset:0\n\t"
    "v_mfma_f32_16x16x32_bf16 v[28:31], a[180:183], v[96:99], v[28:31]\n\t"
    "buffer_load_dwordx4 v2, s[20:23], s88 offen  lds\n\t"
    "v_mfma_f32_16x16x32_bf16 v[28:31], a[184:187], v[100:103], v[28:31]\n\t"
    "ds_read_b128 v[72:75], v11 offset:5120\n\t"
    "ds_read_b64_tr_b16 v[226:227], v6 offset:256\n\t"
    "v_mfma_f32_16x16x32_bf16 v[28:31], a[188:191], v[104:107], v[28:31]\n\t"
    "s_add_u32 m0, s43, 9216\n\t"
    "v_mfma_f32_16x16x32_bf16 v[28:31], a[192:195], v[108:111], v[28:31]\n\t"
    "s_waitcnt vmcnt(5)\n\t"
    "buffer_load_dwordx4 v2, s[20:23], s89 offen  lds\n\t"
    "v_mfma_f32_16x16x32_bf16 v[32:35], v[136:139], v[84:87], 0\n\t"
    "ds_read_b128 v[76:79], v10 offset:6144\n\t"
    "ds_read_b64_tr_b16 v[172:173], v5 offset:2048\n\t"
    "v_mfma_f32_16x16x32_bf16 v[32:35], v[140:143], v[88:91], v[32:35]\n\t"
    "s_waitcnt lgkmcnt(14)\n\t"
    "ds_read_b64_tr_b16 v[174:175], v5 offset:2304\n\t"
    "v_mfma_f32_16x16x32_bf16 v[32:35], v[144:147], v[92:95], v[32:35]\n\t"
    "s_waitcnt lgkmcnt(14)\n\t"
    "ds_read_b64_tr_b16 v[228:229], v6 offset:2048\n\t"
    "v_mfma_f32_16x16x32_bf16 v[32:35], v[148:151], v[96:99], v[32:35]\n\t"
    "s_waitcnt lgkmcnt(14)\n\t"
    "ds_read_b128 v[80:83], v11 offset:6144\n\t"
    "v_mfma_f32_16x16x32_bf16 v[32:35], v[152:155], v[100:103], v[32:35]\n\t"
    "s_add_u32 m0, s43, 10240\n\t"
    "s_waitcnt lgkmcnt(14)\n\t"
    "ds_read_b64_tr_b16 v[230:231], v6 offset:2304\n\t"
    "v_mfma_f32_16x16x32_bf16 v[32:35], v[156:159], v[104:107], v[32:35]\n\t"
    "s_waitcnt lgkmcnt(12)\n\t"
    "buffer_load_dwordx4 v2, s[20:23], s90 offen  lds\n\t"
    "v_mfma_f32_16x16x32_bf16 v[32:35], v[160:163], v[108:111], v[32:35]\n\t"
    "s_waitcnt vmcnt(5)\n\t"
    "ds_read_b64_tr_b16 v[176:177], v5 offset:4096\n\t"
    "ds_read_b64_tr_b16 v[178:179], v5 offset:4352\n\t"
    "v_mfma_f32_16x16x32_bf16 v[36:39], v[192:195], v[84:87], 0\n\t"
    "s_add_u32 m0, s43, 11264\n\t"
    "ds_read_b64_tr_b16 v[232:233], v6 offset:4096\n\t"
    "v_mfma_f32_16x16x32_bf16 v[36:39], v[196:199], v[88:91], v[36:39]\n\t"
    "s_waitcnt lgkmcnt(14)\n\t"
    "buffer_load_dwordx4 v2, s[20:23], s91 offen  lds\n\t"
    "v_mfma_f32_16x16x32_bf16 v[36:39], v[200:203], v[92:95], v[36:39]\n\t"
    "ds_read_b64_tr_b16 v[234:235], v6 offset:4352\n\t"
    "v_mfma_f32_16x16x32_bf16 v[36:39], v[204:207], v[96:99], v[36:39]\n\t"
    "s_waitcnt vmcnt(4) lgkmcnt(14)\n\t"
    "ds_read_b64_tr_b16 v[180:181], v5 offset:6144\n\t"
    "v_mfma_f32_16x16x32_bf16 v[36:39], v[208:211], v[100:103], v[36:39]\n\t"
    "s_waitcnt lgkmcnt(14)\n\t"
    "ds_read_b64_tr_b16 v[182:183], v5 offset:6400\n\t"
    "v_mfma_f32_16x16x32_bf16 v[36:39], v[212:215], v[104:107], v[36:39]\n\t"
    "s_waitcnt lgkmcnt(14)\n\t"
    "ds_read_b64_tr_b16 v[236:237], v6 offset:6144\n\t"
    "v_mfma_f32_16x16x32_bf16 v[36:39], v[216:219], v[108:111], v[36:39]\n\t"
    "s_waitcnt lgkmcnt(14)\n\t"
    "ds_read_b64_tr_b16 v[238:239], v6 offset:6400\n\t"
    "v_mfma_f32_16x16x32_bf16 v[40:43], a[140:143], v[56:59], v[40:43]\n\t"
    "s_waitcnt vmcnt(2) lgkmcnt(14)\n\t"
    "ds_read_b128 v[84:87], v11 offset:31744\n\t"
    "v_mfma_f32_16x16x32_bf16 v[40:43], a[144:147], v[60:63], v[40:43]\n\t"
    "s_waitcnt lgkmcnt(14)\n\t"
    "buffer_store_dwordx4 v[132:135], v16, s[24:27], s68 offen nt\n\t"
    "v_mfma_f32_16x16x32_bf16 v[40:43], a[148:151], v[64:67], v[40:43]\n\t"
    "ds_read_b128 v[88:91], v10 offset:32768\n\t"
    "v_mfma_f32_16x16x32_bf16 v[40:43], a[152:155], v[68:71], v[40:43]\n\t"
    "s_waitcnt lgkmcnt(14)\n\t"
    "ds_read_b64_tr_b16 v[184:185], v5 offset:8192\n\t"
    "v_mfma_f32_16x16x32_bf16 v[40:43], a[156:159], v[72:75], v[40:43]\n\t"
    "s_waitcnt lgkmcnt(14)\n\t"
    "ds_read_b64_tr_b16 v[186:187], v5 offset:8448\n\t"
    "v_mfma_f32_16x16x32_bf16 v[40:43], a[160:163], v[76:79], v[40:43]\n\t"
    "s_waitcnt lgkmcnt(13)\n\t"
    "ds_read_b128 v[92:95], v11 offset:32768\n\t"
    "ds_read_b64_tr_b16 v[240:241], v6 offset:8192\n\t"
    "v_mfma_f32_16x16x32_bf16 v[40:43], a[164:167], v[80:83], v[40:43]\n\t"
    "s_waitcnt lgkmcnt(14)\n\t"
    "ds_read_b64_tr_b16 v[242:243], v6 offset:8448\n\t"
    "v_mfma_f32_16x16x32_bf16 v[44:47], a[196:199], v[56:59], v[44:47]\n\t"
    "s_waitcnt vmcnt(1) lgkmcnt(14)\n\t"
    "ds_read_b64_tr_b16 v[188:189], v5 offset:10240\n\t"
    "v_mfma_f32_16x16x32_bf16 v[44:47], a[200:203], v[60:63], v[44:47]\n\t"
    "s_waitcnt lgkmcnt(14)\n\t"
    "ds_read_b128 v[96:99], v10 offset:33792\n\t"
    "v_mfma_f32_16x16x32_bf16 v[44:47], a[204:207], v[64:67], v[44:47]\n\t"
    "s_waitcnt lgkmcnt(14)\n\t"
    "buffer_store_dwordx4 v[248:251], v16, s[24:27], s70 offen nt\n\t"
    "v_mfma_f32_16x16x32_bf16 v[44:47], a[208:211], v[68:71], v[44:47]\n\t"
    "ds_read_b64_tr_b16 v[190:191], v5 offset:10496\n\t"
    "v_mfma_f32_16x16x32_bf16 v[44:47], a[212:215], v[72:75], v[44:47]\n\t"
    "s_waitcnt lgkmcnt(14)\n\t"
    "ds_read_b128 v[100:103], v11 offset:33792\n\t"
    "v_mfma_f32_16x16x32_bf16 v[44:47], a[216:219], v[76:79], v[44:47]\n\t"
    "s_waitcnt lgkmcnt(14)\n\t"
    "ds_read_b64_tr_b16 v[244:245], v6 offset:10240\n\t"
    "v_mfma_f32_16x16x32_bf16 v[44:47], a[220:223], v[80:83], v[44:47]\n\t"
    "s_waitcnt lgkmcnt(14)\n\t"
    "ds_read_b64_tr_b16 v[246:247], v6 offset:10496\n\t"
    "v_mfma_f32_16x16x32_bf16 v[48:51], v[164:167], v[56:59], v[48:51]\n\t"
    "s_waitcnt lgkmcnt(14)\n\t"
    "ds_read_b128 v[104:107], v10 offset:34816\n\t"
    "v_mfma_f32_16x16x32_bf16 v[48:51], v[168:171], v[60:63], v[48:51]\n\t"
    "v_mfma_f32_16x16x32_bf16 v[48:51], v[172:175], v[64:67], v[48:51]\n\t"
    "v_cvt_pk_bf16_f32 v20, v40, v41\n\t"
    "v_cvt_pk_bf16_f32 v21, v42, v43\n\t"
    "v_mfma_f32_16x16x32_bf16 v[48:51], v[176:179], v[68:71], v[48:51]\n\t"
    "s_waitcnt lgkmcnt(14)\n\t"
    "ds_read_b128 v[108:111], v11 offset:34816\n\t"
    "v_cvt_pk_bf16_f32 v22, v44, v45\n\t"
    "v_mfma_f32_16x16x32_bf16 v[48:51], v[180:183], v[72:75], v[48:51]\n\t"
    "s_waitcnt lgkmcnt(11)\n\t"
    "buffer_store_dwordx4 v[112:115], v16, s[24:27], s70 offen offset:256 nt\n\t"
    "v_mfma_f32_16x16x32_bf16 v[48:51], v[184:187], v[76:79], v[48:51]\n\t"
    "s_waitcnt lgkmcnt(5)\n\t"
    "v_cvt_pk_bf16_f32 v23, v46, v47\n\t"
    "v_cndmask_b32_dpp v124, v22, v20, vcc quad_perm:[1,0,3,2] row_mask:0xf bank_mask:0xf\n\t"
    "v_mfma_f32_16x16x32_bf16 v[48:51], v[188:191], v[80:83], v[48:51]\n\t"
    "v_cndmask_b32_dpp v125, v23, v21, vcc quad_perm:[1,0,3,2] row_mask:0xf bank_mask:0xf\n\t"
    "s_mov_b64 vcc, s[34:35]\n\t"
    "v_mfma_f32_16x16x32_bf16 v[52:55], v[220:223], v[56:59], v[52:55]\n\t"
    "v_cndmask_b32_dpp v126, v20, v22, vcc quad_perm:[1,0,3,2] row_mask:0xf bank_mask:0xf\n\t"
    "v_cndmask_b32_dpp v127, v21, v23, vcc quad_perm:[1,0,3,2] row_mask:0xf bank_mask:0xf\n\t"
    "v_mfma_f32_16x16x32_bf16 v[52:55], v[224:227], v[60:63], v[52:55]\n\t"
    "v_permlane16_swap_b32 v124, v126\n\t"
    "v_permlane16_swap_b32 v125, v127\n\t"
    "v_mfma_f32_16x16x32_bf16 v[52:55], v[228:231], v[64:67], v[52:55]\n\t"
    "v_mfma_f32_16x16x32_bf16 v[52:55], v[232:235], v[68:71], v[52:55]\n\t"
    "v_mfma_f32_16x16x32_bf16 v[52:55], v[236:239], v[72:75], v[52:55]\n\t"
    "v_add_u32 v2, s60, v2\n\t"
    "s_add_u32 m0, s43, 0\n\t"
    "v_mfma_f32_16x16x32_bf16 v[52:55], v[240:243], v[76:79], v[52:55]\n\t"
    "s_waitcnt lgkmcnt(2)\n\t"
    "buffer_load_dwordx4 v2, s[20:23], s64 offen  lds\n\t"
    "v_mfma_f32_16x16x32_bf16 v[52:55], v[244:247], v[80:83], v[52:55]\n\t"
    "v_mfma_f32_16x16x32_bf16 v[24:27], a[140:143], v[84:87], v[24:27]\n\t"
    "s_add_u32 m0, s43, 1024\n\t"
    "ds_read_b128 v[56:59], v14 offset:0\n\t"
    "v_mfma_f32_16x16x32_bf16 v[24:27], a[144:147], v[88:91], v[24:27]\n\t"
    "buffer_load_dwordx4 v2, s[20:23], s65 offen  lds\n\t"
    "v_mfma_f32_16x16x32_bf16 v[24:27], a[148:151], v[92:95], v[24:27]\n\t"
    "ds_read_b128 v[60:63], v15 offset:0\n\t"
    "v_cvt_pk_bf16_f32 v20, v48, v49\n\t"
    "v_mfma_f32_16x16x32_bf16 v[24:27], a[152:155], v[96:99], v[24:27]\n\t"
    "s_waitcnt lgkmcnt(3)\n\t"
    "v_cvt_pk_bf16_f32 v21, v50, v51\n\t"
    "v_cvt_pk_bf16_f32 v22, v52, v53\n\t"
    "v_mfma_f32_16x16x32_bf16 v[24:27], a[156:159], v[100:103], v[24:27]\n\t"
    "v_cvt_pk_bf16_f32 v23, v54, v55\n\t"
    "v_cndmask_b32_dpp v130, v20, v22, vcc quad_perm:[1,0,3,2] row_mask:0xf bank_mask:0xf\n\t"
    "v_mfma_f32_16x16x32_bf16 v[24:27], a[160:163], v[104:107], v[24:27]\n\t"
    "s_waitcnt lgkmcnt(2)\n\t"
    "ds_read_b128 v[64:67], v14 offset:1024\n\t"
    "s_add_u32 m0, s43, 2048\n\t"
    "v_mfma_f32_16x16x32_bf16 v[24:27], a[164:167], v[108:111], v[24:27]\n\t"
    "buffer_load_dwordx4 v2, s[20:23], s66 offen  lds\n\t"
    "v_mfma_f32_16x16x32_bf16 v[28:31], a[196:199], v[84:87], v[28:31]\n\t"
    "v_cndmask_b32_dpp v131, v21, v23, vcc quad_perm:[1,0,3,2] row_mask:0xf bank_mask:0xf\n\t"
    "s_mov_b64 vcc, s[46:47]\n\t"
    "v_mfma_f32_16x16x32_bf16 v[28:31], a[200:203], v[88:91], v[28:31]\n\t"
    "ds_read_b128 v[68:71], v15 offset:1024\n\t"
    "v_cndmask_b32_dpp v128, v22, v20, vcc quad_perm:[1,0,3,2] row_mask:0xf bank_mask:0xf\n\t"
    "v_mfma_f32_16x16x32_bf16 v[28:31], a[204:207], v[92:95], v[28:31]\n\t"
    "s_add_u32 m0, s43, 3072\n\t"
    "v_cndmask_b32_dpp v129, v23, v21, vcc quad_perm:[1,0,3,2] row_mask:0xf bank_mask:0xf\n\t"
    "v_permlane16_swap_b32 v128, v130\n\t"
    "v_mfma_f32_16x16x32_bf16 v[28:31], a[208:211], v[96:99], v[28:31]\n\t"
    "buffer_load_dwordx4 v2, s[20:23], s67 offen  lds\n\t"
    "v_mfma_f32_16x16x32_bf16 v[28:31], a[212:215], v[100:103], v[28:31]\n\t"
    "ds_read_b128 v[72:75], v14 offset:2048\n\t"
    "v_permlane16_swap_b32 v129, v131\n\t"
    "v_mfma_f32_16x16x32_bf16 v[28:31], a[216:219], v[104:107], v[28:31]\n\t"
    "v_mfma_f32_16x16x32_bf16 v[28:31], a[220:223], v[108:111], v[28:31]\n\t"
    "v_mfma_f32_16x16x32_bf16 v[32:35], v[164:167], v[84:87], v[32:35]\n\t"
    "s_add_u32 m0, s43, 4096\n\t"
    "ds_read_b128 v[76:79], v15 offset:2048\n\t"
    "v_mfma_f32_16x16x32_bf16 v[32:35], v[168:171], v[88:91], v[32:35]\n\t"
    "buffer_load_dwordx4 v2, s[20:23], s68 offen  lds\n\t"
    "v_mfma_f32_16x16x32_bf16 v[32:35], v[172:175], v[92:95], v[32:35]\n\t"
    "v_cvt_pk_bf16_f32 v20, v24, v25\n\t"
    "v_cvt_pk_bf16_f32 v21, v26, v27\n\t"
    "v_mfma_f32_16x16x32_bf16 v[32:35], v[176:179], v[96:99], v[32:35]\n\t"
    "s_add_u32 m0, s43, 5120\n\t"
    "ds_read_b128 v[80:83], v14 offset:3072\n\t"
    "v_cvt_pk_bf16_f32 v22, v28, v29\n\t"
    "v_mfma_f32_16x16x32_bf16 v[32:35], v[180:183], v[100:103], v[32:35]\n\t"
    "buffer_load_dwordx4 v2, s[20:23], s69 offen  lds\n\t"
    "v_mfma_f32_16x16x32_bf16 v[32:35], v[184:187], v[104:107], v[32:35]\n\t"
    "v_cvt_pk_bf16_f32 v23, v30, v31\n\t"
    "v_cndmask_b32_dpp v132, v22, v20, vcc quad_perm:[1,0,3,2] row_mask:0xf bank_mask:0xf\n\t"
    "v_mfma_f32_16x16x32_bf16 v[32:35], v[188:191], v[108:111], v[32:35]\n\t"
    "v_cndmask_b32_dpp v133, v23, v21, vcc quad_perm:[1,0,3,2] row_mask:0xf bank_mask:0xf\n\t"
    "s_mov_b64 vcc, s[34:35]\n\t"
    "v_mfma_f32_16x16x32_bf16 v[36:39], v[220:223], v[84:87], v[36:39]\n\t"
    "v_cndmask_b32_dpp v134, v20, v22, vcc quad_perm:[1,0,3,2] row_mask:0xf bank_mask:0xf\n\t"
    "v_cndmask_b32_dpp v135, v21, v23, vcc quad_perm:[1,0,3,2] row_mask:0xf bank_mask:0xf\n\t"
    "v_mfma_f32_16x16x32_bf16 v[36:39], v[224:227], v[88:91], v[36:39]\n\t"
    "s_add_u32 m0, s43, 6144\n\t"
    "v_permlane16_swap_b32 v132, v134\n\t"
    "v_permlane16_swap_b32 v133, v135\n\t"
    "v_mfma_f32_16x16x32_bf16 v[36:39], v[228:231], v[92:95], v[36:39]\n\t"
    "buffer_load_dwordx4 v2, s[20:23], s70 offen  lds\n\t"
    "v_mfma_f32_16x16x32_bf16 v[36:39], v[232:235], v[96:99], v[36:39]\n\t"
    "v_mfma_f32_16x16x32_bf16 v[36:39], v[236:239], v[100:103], v[36:39]\n\t"
    "s_add_u32 m0, s43, 7168\n\t"
    "v_mfma_f32_16x16x32_bf16 v[36:39], v[240:243], v[104:107], v[36:39]\n\t"
    "buffer_load_dwordx4 v2, s[20:23], s71 offen  lds\n\t"
    "v_mfma_f32_16x16x32_bf16 v[36:39], v[244:247], v[108:111], v[36:39]\n\t"
    "s_waitcnt lgkmcnt(5)\n\t"
    "v_mfma_f32_16x16x32_bf16 v[40:43], a[112:115], v[56:59], 0\n\t"
    "ds_read_b128 v[84:87], v15 offset:3072\n\t"
    "v_mfma_f32_16x16x32_bf16 v[40:43], a[116:119], v[60:63], v[40:43]\n\t"
    "s_waitcnt lgkmcnt(4)\n\t"
    "v_mfma_f32_16x16x32_bf16 v[40:43], a[120:123], v[64:67], v[40:43]\n\t"
    "ds_read_b128 v[88:91], v14 offset:4096\n\t"
    "v_cvt_pk_bf16_f32 v20, v32, v33\n\t"
    "v_mfma_f32_16x16x32_bf16 v[40:43], a[124:127], v[68:71], v[40:43]\n\t"
    "s_waitcnt lgkmcnt(3)\n\t"
    "s_add_u32 m0, s43, 8192\n\t"
    "v_cvt_pk_bf16_f32 v21, v34, v35\n\t"
    "v_mfma_f32_16x16x32_bf16 v[40:43], a[128:131], v[72:75], v[40:43]\n\t"
    "buffer_load_dwordx4 v2, s[20:23], s72 offen  lds\n\t"
    "v_mfma_f32_16x16x32_bf16 v[40:43], a[132:135], v[76:79], v[40:43]\n\t"
    "s_waitcnt lgkmcnt(2)\n\t"
    "ds_read_b128 v[92:95], v15 offset:4096\n\t"
    "v_cvt_pk_bf16_f32 v22, v36, v37\n\t"
    "v_mfma_f32_16x16x32_bf16 v[40:43], a[136:139], v[80:83], v[40:43]\n\t"
    "s_add_u32 m0, s43, 9216\n\t"
    "v_cvt_pk_bf16_f32 v23, v38, v39\n\t"
    "v_cndmask_b32_dpp v250, v20, v22, vcc quad_perm:[1,0,3,2] row_mask:0xf bank_mask:0xf\n\t"
    "v_mfma_f32_16x16x32_bf16 v[44:47], a[168:171], v[56:59], 0\n\t"
    "s_waitcnt vmcnt(7)\n\t"
    "buffer_load_dwordx4 v2, s[20:23], s73 offen  lds\n\t"
    "v_mfma_f32_16x16x32_bf16 v[44:47], a[172:175], v[60:63], v[44:47]\n\t"
    "ds_read_b128 v[96:99], v14 offset:5120\n\t"
    "v_cndmask_b32_dpp v251, v21, v23, vcc quad_perm:[1,0,3,2] row_mask:0xf bank_mask:0xf\n\t"
    "v_mfma_f32_16x16x32_bf16 v[44:47], a[176:179], v[64:67], v[44:47]\n\t"
    "s_mov_b64 vcc, s[46:47]\n\t"
    "v_cndmask_b32_dpp v248, v22, v20, vcc quad_perm:[1,0,3,2] row_mask:0xf bank_mask:0xf\n\t"
    "v_cndmask_b32_dpp v249, v23, v21, vcc quad_perm:[1,0,3,2] row_mask:0xf bank_mask:0xf\n\t"
    "v_mfma_f32_16x16x32_bf16 v[44:47], a[180:183], v[68:71], v[44:47]\n\t"
    "v_permlane16_swap_b32 v248, v250\n\t"
    "v_permlane16_swap_b32 v249, v251\n\t"
    "v_mfma_f32_16x16x32_bf16 v[44:47], a[184:187], v[72:75], v[44:47]\n\t"
    "s_add_u32 m0, s43, 10240\n\t"
    "ds_read_b128 v[100:103], v15 offset:5120\n\t"
    "ds_read_b64_tr_b16 a[0:1], v5 offset:0\n\t"
    "v_mfma_f32_16x16x32_bf16 v[44:47], a[188:191], v[76:79], v[44:47]\n\t"
    "buffer_load_dwordx4 v2, s[20:23], s74 offen  lds\n\t"
    "v_mfma_f32_16x16x32_bf16 v[44:47], a[192:195], v[80:83], v[44:47]\n\t"
    "ds_read_b64_tr_b16 a[2:3], v5 offset:256\n\t"
    "ds_read_b64_tr_b16 a[56:57], v6 offset:0\n\t"
    "v_mfma_f32_16x16x32_bf16 v[48:51], v[136:139], v[56:59], 0\n\t"
    "s_add_u32 m0, s43, 11264\n\t"
    "ds_read_b128 v[104:107], v14 offset:6144\n\t"
    "ds_read_b64_tr_b16 a[58:59], v6 offset:256\n\t"
    "v_mfma_f32_16x16x32_bf16 v[48:51], v[140:143], v[60:63], v[48:51]\n\t"
    "s_waitcnt vmcnt(7)\n\t"
    "buffer_load_dwordx4 v2, s[20:23], s75 offen  lds\n\t"
    "v_mfma_f32_16x16x32_bf16 v[48:51], v[144:147], v[64:67], v[48:51]\n\t"
    "ds_read_b64_tr_b16 a[4:5], v5 offset:2048\n\t"
    "ds_read_b64_tr_b16 a[6:7], v5 offset:2304\n\t"
    "v_mfma_f32_16x16x32_bf16 v[48:51], v[148:151], v[68:71], v[48:51]\n\t"
    "ds_read_b128 v[108:111], v15 offset:6144\n\t"
    "ds_read_b64_tr_b16 a[60:61], v6 offset:2048\n\t"
    "v_mfma_f32_16x16x32_bf16 v[48:51], v[152:155], v[72:75], v[48:51]\n\t"
    "ds_read_b64_tr_b16 a[62:63], v6 offset:2304\n\t"
    "v_mfma_f32_16x16x32_bf16 v[48:51], v[156:159], v[76:79], v[48:51]\n\t"
    "v_mfma_f32_16x16x32_bf16 v[48:51], v[160:163], v[80:83], v[48:51]\n\t"
    "s_add_u32 m0, s43, 0\n\t"
    "v_mfma_f32_16x16x32_bf16 v[52:55], v[192:195], v[56:59], 0\n\t"
    "s_waitcnt lgkmcnt(5)\n\t"
    "buffer_load_dwordx4 v2, s[20:23], s76 offen  lds\n\t"
    "v_mfma_f32_16x16x32_bf16 v[52:55], v[196:199], v[60:63], v[52:55]\n\t"
    "s_waitcnt vmcnt(7)\n\t"
    "ds_read_b64_tr_b16 a[8:9], v5 offset:4096\n\t"
    "ds_read_b64_tr_b16 a[10:11], v5 offset:4352\n\t"
    "v_mfma_f32_16x16x32_bf16 v[52:55], v[200:203], v[64:67], v[52:55]\n\t"
    "s_add_u32 m0, s43, 1024\n\t"
    "ds_read_b64_tr_b16 a[64:65], v6 offset:4096\n\t"
    "ds_read_b64_tr_b16 a[66:67], v6 offset:4352\n\t"
    "v_mfma_f32_16x16x32_bf16 v[52:55], v[204:207], v[68:71], v[52:55]\n\t"
    "buffer_load_dwordx4 v2, s[20:23], s77 offen  lds\n\t"
    "v_mfma_f32_16x16x32_bf16 v[52:55], v[208:211], v[72:75], v[52:55]\n\t"
    "v_mfma_f32_16x16x32_bf16 v[52:55], v[212:215], v[76:79], v[52:55]\n\t"
    "v_mfma_f32_16x16x32_bf16 v[52:55], v[216:219], v[80:83], v[52:55]\n\t"
    "v_mfma_f32_16x16x32_bf16 v[40:43], a[140:143], v[84:87], v[40:43]\n\t"
    "s_add_u32 m0, s43, 2048\n\t"
    "ds_read_b128 v[56:59], v8 offset:0\n\t"
    "v_mfma_f32_16x16x32_bf16 v[40:43], a[144:147], v[88:91], v[40:43]\n\t"
    "s_waitcnt lgkmcnt(5)\n\t"
    "buffer_load_dwordx4 v2, s[20:23], s78 offen  lds\n\t"
    "v_mfma_f32_16x16x32_bf16 v[40:43], a[148:151], v[92:95], v[40:43]\n\t"
    "ds_read_b128 v[60:63], v9 offset:0\n\t"
    "v_mfma_f32_16x16x32_bf16 v[40:43], a[152:155], v[96:99], v[40:43]\n\t"
    "s_add_u32 m0, s43, 3072\n\t"
    "s_waitcnt vmcnt(7)\n\t"
    "ds_read_b64_tr_b16 a[12:13], v5 offset:6144\n\t"
    "v_mfma_f32_16x16x32_bf16 v[40:43], a[156:159], v[100:103], v[40:43]\n\t"
    "buffer_load_dwordx4 v2, s[20:23], s79 offen  lds\n\t"
    "v_mfma_f32_16x16x32_bf16 v[40:43], a[160:163], v[104:107], v[40:43]\n\t"
    "ds_read_b128 v[64:67], v8 offset:1024\n\t"
    "ds_read_b64_tr_b16 a[14:15], v5 offset:6400\n\t"
    "v_mfma_f32_16x16x32_bf16 v[40:43], a[164:167], v[108:111], v[40:43]\n\t"
    "ds_read_b64_tr_b16 a[68:69], v6 offset:6144\n\t"
    "ds_read_b64_tr_b16 a[70:71], v6 offset:6400\n\t"
    "v_mfma_f32_16x16x32_bf16 v[44:47], a[196:199], v[84:87], v[44:47]\n\t"
    "v_mfma_f32_16x16x32_bf16 v[44:47], a[200:203], v[88:91], v[44:47]\n\t"
    "s_add_u32 m0, s43, 4096\n\t"
    "ds_read_b128 v[68:71], v9 offset:1024\n\t"
    "v_mfma_f32_16x16x32_bf16 v[44:47], a[204:207], v[92:95], v[44:47]\n\t"
    "s_waitcnt lgkmcnt(8)\n\t"
    "buffer_load_dwordx4 v2, s[20:23], s80 offen  lds\n\t"
    "v_mfma_f32_16x16x32_bf16 v[44:47], a[208:211], v[96:99], v[44:47]\n\t"
    "v_mfma_f32_16x16x32_bf16 v[44:47], a[212:215], v[100:103], v[44:47]\n\t"
    "s_add_u32 m0, s43, 5120\n\t"
    "ds_read_b128 v[72:75], v8 offset:2048\n\t"
    "v_mfma_f32_16x16x32_bf16 v[44:47], a[216:219], v[104:107], v[44:47]\n\t"
    "s_waitcnt vmcnt(7)\n\t"
    "buffer_load_dwordx4 v2, s[20:23], s81 offen  lds\n\t"
    "v_mfma_f32_16x16x32_bf16 v[44:47], a[220:223], v[108:111], v[44:47]\n\t"
    "ds_read_b64_tr_b16 a[16:17], v5 offset:8192\n\t"
    "ds_read_b64_tr_b16 a[18:19], v5 offset:8448\n\t"
    "v_mfma_f32_16x16x32_bf16 v[48:51], v[164:167], v[84:87], v[48:51]\n\t"
    "ds_read_b128 v[76:79], v9 offset:2048\n\t"
    "ds_read_b64_tr_b16 a[72:73], v6 offset:8192\n\t"
    "v_mfma_f32_16x16x32_bf16 v[48:51], v[168:171], v[88:91], v[48:51]\n\t"
    "ds_read_b64_tr_b16 a[74:75], v6 offset:8448\n\t"
    "v_mfma_f32_16x16x32_bf16 v[48:51], v[172:175], v[92:95], v[48:51]\n\t"
    "v_cvt_pk_bf16_f32 v20, v40, v41\n\t"
    "v_cvt_pk_bf16_f32 v21, v42, v43\n\t"
    "v_mfma_f32_16x16x32_bf16 v[48:51], v[176:179], v[96:99], v[48:51]\n\t"
    "s_add_u32 m0, s43, 6144\n\t"
    "ds_read_b128 v[80:83], v8 offset:3072\n\t"
    "v_cvt_pk_bf16_f32 v22, v44, v45\n\t"
    "v_mfma_f32_16x16x32_bf16 v[48:51], v[180:183], v[100:103], v[48:51]\n\t"
    "s_waitcnt lgkmcnt(8)\n\t"
    "buffer_load_dwordx4 v2, s[20:23], s82 offen  lds\n\t"
    "v_mfma_f32_16x16x32_bf16 v[48:51], v[184:187], v[104:107], v[48:51]\n\t"
    "s_waitcnt vmcnt(7)\n\t"
    "v_cvt_pk_bf16_f32 v23, v46, v47\n\t"
    "v_cndmask_b32_dpp v112, v22, v20, vcc quad_perm:[1,0,3,2] row_mask:0xf bank_mask:0xf\n\t"
    "v_mfma_f32_16x16x32_bf16 v[48:51], v[188:191], v[108:111], v[48:51]\n\t"
    "s_add_u32 m0, s43, 7168\n\t"
    "v_cndmask_b32_dpp v113, v23, v21, vcc quad_perm:[1,0,3,2] row_mask:0xf bank_mask:0xf\n\t"
    "s_mov_b64 vcc, s[34:35]\n\t"
    "v_mfma_f32_16x16x32_bf16 v[52:55], v[220:223], v[84:87], v[52:55]\n\t"
    "buffer_load_dwordx4 v2, s[20:23], s83 offen  lds\n\t"
    "v_mfma_f32_16x16x32_bf16 v[52:55], v[224:227], v[88:91], v[52:55]\n\t"
    "v_cndmask_b32_dpp v114, v20, v22, vcc quad_perm:[1,0,3,2] row_mask:0xf bank_mask:0xf\n\t"
    "v_cndmask_b32_dpp v115, v21, v23, vcc quad_perm:[1,0,3,2] row_mask:0xf bank_mask:0xf\n\t"
    "v_mfma_f32_16x16x32_bf16 v[52:55], v[228:231], v[92:95], v[52:55]\n\t"
    "v_permlane16_swap_b32 v112, v114\n\t"
    "v_permlane16_swap_b32 v113, v115\n\t"
    "v_mfma_f32_16x16x32_bf16 v[52:55], v[232:235], v[96:99], v[52:55]\n\t"
    "buffer_store_dwordx4 v[116:119], v17, s[24:27], s70 offen nt\n\t"
    "v_mfma_f32_16x16x32_bf16 v[52:55], v[236:239], v[100:103], v[52:55]\n\t"
    "ds_read_b64_tr_b16 a[20:21], v5 offset:10240\n\t"
    "ds_read_b64_tr_b16 a[22:23], v5 offset:10496\n\t"
    "v_mfma_f32_16x16x32_bf16 v[52:55], v[240:243], v[104:107], v[52:55]\n\t"
    "ds_read_b64_tr_b16 a[76:77], v6 offset:10240\n\t"
    "ds_read_b64_tr_b16 a[78:79], v6 offset:10496\n\t"
    "v_mfma_f32_16x16x32_bf16 v[52:55], v[244:247], v[108:111], v[52:55]\n\t"
    "v_mfma_f32_16x16x32_bf16 v[24:27], a[112:115], v[56:59], 0\n\t"
    "s_waitcnt vmcnt(7)\n\t"
    "ds_read_b128 v[84:87], v9 offset:3072\n\t"
    "ds_read_b64_tr_b16 a[24:25], v5 offset:0\n\t"
    "v_mfma_f32_16x16x32_bf16 v[24:27], a[116:119], v[60:63], v[24:27]\n\t"
    "s_waitcnt lgkmcnt(13)\n\t"
    "ds_read_b64_tr_b16 a[26:27], v5 offset:256\n\t"
    "ds_read_b64_tr_b16 a[80:81], v6 offset:0\n\t"
    "v_mfma_f32_16x16x32_bf16 v[24:27], a[120:123], v[64:67], v[24:27]\n\t"
    "s_waitcnt lgkmcnt(14)\n\t"
    "v_cvt_pk_bf16_f32 v20, v48, v49\n\t"
    "v_cvt_pk_bf16_f32 v21, v50, v51\n\t"
    "v_mfma_f32_16x16x32_bf16 v[24:27], a[124:127], v[68:71], v[24:27]\n\t"
    "s_waitcnt lgkmcnt(11)\n\t"
    "v_cvt_pk_bf16_f32 v22, v52, v53\n\t"
    "v_cvt_pk_bf16_f32 v23, v54, v55\n\t"
    "v_mfma_f32_16x16x32_bf16 v[24:27], a[128:131], v[72:75], v[24:27]\n\t"
    "ds_read_b128 v[88:91], v8 offset:4096\n\t"
    "v_cndmask_b32_dpp v118, v20, v22, vcc quad_perm:[1,0,3,2] row_mask:0xf bank_mask:0xf\n\t"
    "v_mfma_f32_16x16x32_bf16 v[24:27], a[132:135], v[76:79], v[24:27]\n\t"
    "s_waitcnt lgkmcnt(9)\n\t"
    "v_cndmask_b32_dpp v119, v21, v23, vcc quad_perm:[1,0,3,2] row_mask:0xf bank_mask:0xf\n\t"
    "s_mov_b64 vcc, s[46:47]\n\t"
    "v_mfma_f32_16x16x32_bf16 v[24:27], a[136:139], v[80:83], v[24:27]\n\t"
    "s_add_u32 m0, s43, 8192\n\t"
    "v_cndmask_b32_dpp v116, v22, v20, vcc quad_perm:[1,0,3,2] row_mask:0xf bank_mask:0xf\n\t"
    "v_cndmask_b32_dpp v117, v23, v21, vcc quad_perm:[1,0,3,2] row_mask:0xf bank_mask:0xf\n\t"
    "v_mfma_f32_16x16x32_bf16 v[28:31], a[168:171], v[56:59], 0\n\t"
    "buffer_load_dwordx4 v2, s[20:23], s84 offen  lds\n\t"
    "v_mfma_f32_16x16x32_bf16 v[28:31], a[172:175], v[60:63], v[28:31]\n\t"
    "ds_read_b128 v[92:95], v9 offset:4096\n\t"
    "v_permlane16_swap_b32 v116, v118\n\t"
    "v_mfma_f32_16x16x32_bf16 v[28:31], a[176:179], v[64:67], v[28:31]\n\t"
    "s_add_u32 m0, s43, 9216\n\t"
    "v_permlane16_swap_b32 v117, v119\n\t"
    "ds_read_b64_tr_b16 a[82:83], v6 offset:256\n\t"
    "v_mfma_f32_16x16x32_bf16 v[28:31], a[180:183], v[68:71], v[28:31]\n\t"
    "s_waitcnt vmcnt(6)\n\t"
    "buffer_load_dwordx4 v2, s[20:23], s85 offen  lds\n\t"
    "v_mfma_f32_16x16x32_bf16 v[28:31], a[184:187], v[72:75], v[28:31]\n\t"
    "ds_read_b64_tr_b16 a[28:29], v5 offset:2048\n\t"
    "ds_read_b64_tr_b16 a[30:31], v5 offset:2304\n\t"
    "v_mfma_f32_16x16x32_bf16 v[28:31], a[188:191], v[76:79], v[28:31]\n\t"
    "ds_read_b128 v[96:99], v8 offset:5120\n\t"
    "ds_read_b64_tr_b16 a[84:85], v6 offset:2048\n\t"
    "v_mfma_f32_16x16x32_bf16 v[28:31], a[192:195], v[80:83], v[28:31]\n\t"
    "s_waitcnt lgkmcnt(14)\n\t"
    "buffer_store_dwordx4 v[120:123], v17, s[24:27], s70 offen offset:256 nt\n\t"
    "v_mfma_f32_16x16x32_bf16 v[32:35], v[136:139], v[56:59], 0\n\t"
    "ds_read_b64_tr_b16 a[86:87], v6 offset:2304\n\t"
    "v_mfma_f32_16x16x32_bf16 v[32:35], v[140:143], v[60:63], v[32:35]\n\t"
    "v_mfma_f32_16x16x32_bf16 v[32:35], v[144:147], v[64:67], v[32:35]\n\t"
    "v_mfma_f32_16x16x32_bf16 v[32:35], v[148:151], v[68:71], v[32:35]\n\t"
    "s_waitcnt vmcnt(6) lgkmcnt(14)\n\t"
    "ds_read_b128 v[100:103], v9 offset:5120\n\t"
    "v_mfma_f32_16x16x32_bf16 v[32:35], v[152:155], v[72:75], v[32:35]\n\t"
    "s_waitcnt lgkmcnt(14)\n\t"
    "ds_read_b64_tr_b16 a[32:33], v5 offset:4096\n\t"
    "v_mfma_f32_16x16x32_bf16 v[32:35], v[156:159], v[76:79], v[32:35]\n\t"
    "s_waitcnt lgkmcnt(14)\n\t"
    "ds_read_b64_tr_b16 a[34:35], v5 offset:4352\n\t"
    "v_mfma_f32_16x16x32_bf16 v[32:35], v[160:163], v[80:83], v[32:35]\n\t"
    "s_waitcnt lgkmcnt(14)\n\t"
    "ds_read_b64_tr_b16 a[88:89], v6 offset:4096\n\t"
    "v_mfma_f32_16x16x32_bf16 v[36:39], v[192:195], v[56:59], 0\n\t"
    "s_waitcnt lgkmcnt(14)\n\t"
    "ds_read_b128 v[104:107], v8 offset:6144\n\t"
    "v_mfma_f32_16x16x32_bf16 v[36:39], v[196:199], v[60:63], v[36:39]\n\t"
    "s_waitcnt lgkmcnt(14)\n\t"
    "ds_read_b64_tr_b16 a[90:91], v6 offset:4352\n\t"
    "v_mfma_f32_16x16x32_bf16 v[36:39], v[200:203], v[64:67], v[36:39]\n\t"
    "s_add_u32 m0, s43, 10240\n\t"
    "v_mfma_f32_16x16x32_bf16 v[36:39], v[204:207], v[68:71], v[36:39]\n\t"
    "buffer_load_dwordx4 v2, s[20:23], s86 offen  lds\n\t"
    "v_mfma_f32_16x16x32_bf16 v[36:39], v[208:211], v[72:75], v[36:39]\n\t"
    "s_waitcnt vmcnt(5) lgkmcnt(14)\n\t"
    "ds_read_b128 v[108:111], v9 offset:6144\n\t"
    "v_mfma_f32_16x16x32_bf16 v[36:39], v[212:215], v[76:79], v[36:39]\n\t"
    "s_waitcnt lgkmcnt(14)\n\t"
    "ds_read_b64_tr_b16 a[36:37], v5 offset:6144\n\t"
    "v_mfma_f32_16x16x32_bf16 v[36:39], v[216:219], v[80:83], v[36:39]\n\t"
    "s_add_u32 m0, s43, 11264\n\t"
    "s_waitcnt lgkmcnt(14)\n\t"
    "ds_read_b64_tr_b16 a[38:39], v5 offset:6400\n\t"
    "v_mfma_f32_16x16x32_bf16 v[40:43], a[0:3], v[56:59], 0\n\t"
    "s_waitcnt lgkmcnt(14)\n\t"
    "buffer_load_dwordx4 v2, s[20:23], s87 offen  lds\n\t"
    "v_mfma_f32_16x16x32_bf16 v[40:43], a[4:7], v[60:63], v[40:43]\n\t"
    "ds_read_b64_tr_b16 a[92:93], v6 offset:6144\n\t"
    "v_mfma_f32_16x16x32_bf16 v[40:43], a[8:11], v[64:67], v[40:43]\n\t"
    "s_waitcnt lgkmcnt(14)\n\t"
    "ds_read_b64_tr_b16 a[94:95], v6 offset:6400\n\t"
    "v_mfma_f32_16x16x32_bf16 v[40:43], a[12:15], v[68:71], v[40:43]\n\t"
    "v_mfma_f32_16x16x32_bf16 v[40:43], a[16:19], v[72:75], v[40:43]\n\t"
    "s_add_u32 m0, s43, 0\n\t"
    "v_mfma_f32_16x16x32_bf16 v[40:43], a[20:23], v[76:79], v[40:43]\n\t"
    "buffer_load_dwordx4 v2, s[20:23], s88 offen  lds\n\t"
    "v_mfma_f32_16x16x32_bf16 v[40:43], a[24:27], v[80:83], v[40:43]\n\t"
    "v_mfma_f32_16x16x32_bf16 v[44:47], a[56:59], v[56:59], 0\n\t"
    "s_add_u32 m0, s43, 1024\n\t"
    "v_mfma_f32_16x16x32_bf16 v[44:47], a[60:63], v[60:63], v[44:47]\n\t"
    "buffer_load_dwordx4 v2, s[20:23], s89 offen  lds\n\t"
    "v_mfma_f32_16x16x32_bf16 v[44:47], a[64:67], v[64:67], v[44:47]\n\t"
    "v_mfma_f32_16x16x32_bf16 v[44:47], a[68:71], v[68:71], v[44:47]\n\t"
    "v_mfma_f32_16x16x32_bf16 v[44:47], a[72:75], v[72:75], v[44:47]\n\t"
    "v_mfma_f32_16x16x32_bf16 v[44:47], a[76:79], v[76:79], v[44:47]\n\t"
    "s_add_u32 m0, s43, 2048\n\t"
    "v_mfma_f32_16x16x32_bf16 v[44:47], a[80:83], v[80:83], v[44:47]\n\t"
    "s_waitcnt lgkmcnt(11)\n\t"
    "buffer_load_dwordx4 v2, s[20:23], s90 offen  lds\n\t"
    "v_mfma_f32_16x16x32_bf16 v[24:27], a[140:143], v[84:87], v[24:27]\n\t"
    "s_waitcnt vmcnt(6)\n\t"
    "ds_read_b128 v[56:59], v8 offset:28672\n\t"
    "ds_read_b64_tr_b16 a[40:41], v5 offset:8192\n\t"
    "v_mfma_f32_16x16x32_bf16 v[24:27], a[144:147], v[88:91], v[24:27]\n\t"
    "s_add_u32 m0, s43, 3072\n\t"
    "ds_read_b64_tr_b16 a[42:43], v5 offset:8448\n\t"
    "ds_read_b64_tr_b16 a[96:97], v6 offset:8192\n\t"
    "v_mfma_f32_16x16x32_bf16 v[24:27], a[148:151], v[92:95], v[24:27]\n\t"
    "s_waitcnt lgkmcnt(14)\n\t"
    "ds_read_b64_tr_b16 a[98:99], v6 offset:8448\n\t"
    "v_mfma_f32_16x16x32_bf16 v[24:27], a[152:155], v[96:99], v[24:27]\n\t"
    "s_waitcnt lgkmcnt(11)\n\t"
    "buffer_load_dwordx4 v2, s[20:23], s91 offen  lds\n\t"
    "v_mfma_f32_16x16x32_bf16 v[24:27], a[156:159], v[100:103], v[24:27]\n\t"
    "ds_read_b128 v[60:63], v9 offset:28672\n\t"
    "v_mfma_f32_16x16x32_bf16 v[24:27], a[160:163], v[104:107], v[24:27]\n\t"
    "s_waitcnt lgkmcnt(10)\n\t"
    "v_mfma_f32_16x16x32_bf16 v[24:27], a[164:167], v[108:111], v[24:27]\n\t"
    "v_mfma_f32_16x16x32_bf16 v[28:31], a[196:199], v[84:87], v[28:31]\n\t"
    "v_mfma_f32_16x16x32_bf16 v[28:31], a[200:203], v[88:91], v[28:31]\n\t"
    "ds_read_b128 v[64:67], v8 offset:29696\n\t"
    "v_mfma_f32_16x16x32_bf16 v[28:31], a[204:207], v[92:95], v[28:31]\n\t"
    "s_waitcnt vmcnt(4)\n\t"
    "buffer_store_dwordx4 v[124:127], v17, s[24:27], s64 offen offset:256 nt\n\t"
    "v_mfma_f32_16x16x32_bf16 v[28:31], a[208:211], v[96:99], v[28:31]\n\t"
    "ds_read_b64_tr_b16 a[44:45], v5 offset:10240\n\t"
    "ds_read_b64_tr_b16 a[46:47], v5 offset:10496\n\t"
    "v_mfma_f32_16x16x32_bf16 v[28:31], a[212:215], v[100:103], v[28:31]\n\t"
    "ds_read_b64_tr_b16 a[100:101], v6 offset:10240\n\t"
    "ds_read_b64_tr_b16 a[102:103], v6 offset:10496\n\t"
    "v_mfma_f32_16x16x32_bf16 v[28:31], a[216:219], v[104:107], v[28:31]\n\t"
    "s_waitcnt lgkmcnt(14)\n\t"
    "ds_read_b128 v[68:71], v9 offset:29696\n\t"
    "v_mfma_f32_16x16x32_bf16 v[28:31], a[220:223], v[108:111], v[28:31]\n\t"
    "v_mfma_f32_16x16x32_bf16 v[32:35], v[164:167], v[84:87], v[32:35]\n\t"
    "s_waitcnt vmcnt(3) lgkmcnt(14)\n\t"
    "ds_read_b64_tr_b16 a[48:49], v5 offset:0\n\t"
    "v_mfma_f32_16x16x32_bf16 v[32:35], v[168:171], v[88:91], v[32:35]\n\t"
    "s_waitcnt lgkmcnt(14)\n\t"
    "ds_read_b64_tr_b16 a[50:51], v5 offset:256\n\t"
    "v_mfma_f32_16x16x32_bf16 v[32:35], v[172:175], v[92:95], v[32:35]\n\t"
    "s_waitcnt lgkmcnt(14)\n\t"
    "v_cvt_pk_bf16_f32 v20, v24, v25\n\t"
    "v_cvt_pk_bf16_f32 v21, v26, v27\n\t"
    "v_mfma_f32_16x16x32_bf16 v[32:35], v[176:179], v[96:99], v[32:35]\n\t"
    "ds_read_b128 v[72:75], v8 offset:30720\n\t"
    "v_cvt_pk_bf16_f32 v22, v28, v29\n\t"
    "v_mfma_f32_16x16x32_bf16 v[32:35], v[180:183], v[100:103], v[32:35]\n\t"
    "s_waitcnt lgkmcnt(14)\n\t"
    "buffer_store_dwordx4 v[128:131], v17, s[24:27], s64 offen offset:512 nt\n\t"
    "v_mfma_f32_16x16x32_bf16 v[32:35], v[184:187], v[104:107], v[32:35]\n\t"
    "v_cvt_pk_bf16_f32 v23, v30, v31\n\t"
    "v_cndmask_b32_dpp v120, v22, v20, vcc quad_perm:[1,0,3,2] row_mask:0xf bank_mask:0xf\n\t"
    "v_mfma_f32_16x16x32_bf16 v[32:35], v[188:191], v[108:111], v[32:35]\n\t"
    "v_cndmask_b32_dpp v121, v23, v21, vcc quad_perm:[1,0,3,2] row_mask:0xf bank_mask:0xf\n\t"
    "s_mov_b64 vcc, s[34:35]\n\t"
    "v_mfma_f32_16x16x32_bf16 v[36:39], v[220:223], v[84:87], v[36:39]\n\t"
    "ds_read_b128 v[76:79], v9 offset:30720\n\t"
    "v_cndmask_b32_dpp v122, v20, v22, vcc quad_perm:[1,0,3,2] row_mask:0xf bank_mask:0xf\n\t"
    "v_mfma_f32_16x16x32_bf16 v[36:39], v[224:227], v[88:91], v[36:39]\n\t"
    "s_waitcnt lgkmcnt(14)\n\t"
    "v_cndmask_b32_dpp v123, v21, v23, vcc quad_perm:[1,0,3,2] row_mask:0xf bank_mask:0xf\n\t"
    "v_permlane16_swap_b32 v120, v122\n\t"
    "v_mfma_f32_16x16x32_bf16 v[36:39], v[228:231], v[92:95], v[36:39]\n\t"
    "v_permlane16_swap_b32 v121, v123\n\t"
    "ds_read_b64_tr_b16 a[104:105], v6 offset:0\n\t"
    "v_mfma_f32_16x16x32_bf16 v[36:39], v[232:235], v[96:99], v[36:39]\n\t"
    "s_waitcnt lgkmcnt(14)\n\t"
    "ds_read_b64_tr_b16 a[106:107], v6 offset:256\n\t"
    "v_mfma_f32_16x16x32_bf16 v[36:39], v[236:239], v[100:103], v[36:39]\n\t"
    "s_waitcnt vmcnt(2) lgkmcnt(14)\n\t"
    "ds_read_b128 v[80:83], v8 offset:31744\n\t"
    "v_mfma_f32_16x16x32_bf16 v[36:39], v[240:243], v[104:107], v[36:39]\n\t"
    "s_waitcnt lgkmcnt(14)\n\t"
    "ds_read_b64_tr_b16 a[52:53], v5 offset:2048\n\t"
    "v_mfma_f32_16x16x32_bf16 v[36:39], v[244:247], v[108:111], v[36:39]\n\t"
    "s_waitcnt lgkmcnt(14)\n\t"
    "buffer_store_dwordx4 v[132:135], v17, s[24:27], s66 offen offset:256 nt\n\t"
    "v_mfma_f32_16x16x32_bf16 v[40:43], a[28:31], v[84:87], v[40:43]\n\t"
    "ds_read_b64_tr_b16 a[54:55], v5 offset:2304\n\t"
    "v_mfma_f32_16x16x32_bf16 v[40:43], a[32:35], v[88:91], v[40:43]\n\t"
    "s_waitcnt lgkmcnt(14)\n\t"
    "ds_read_b64_tr_b16 a[108:109], v6 offset:2048\n\t"
    "v_mfma_f32_16x16x32_bf16 v[40:43], a[36:39], v[92:95], v[40:43]\n\t"
    "s_waitcnt lgkmcnt(14)\n\t"
    "v_cvt_pk_bf16_f32 v20, v32, v33\n\t"
    "v_cvt_pk_bf16_f32 v21, v34, v35\n\t"
    "v_mfma_f32_16x16x32_bf16 v[40:43], a[40:43], v[96:99], v[40:43]\n\t"
    "s_waitcnt lgkmcnt(13)\n\t"
    "v_cvt_pk_bf16_f32 v22, v36, v37\n\t"
    "v_cvt_pk_bf16_f32 v23, v38, v39\n\t"
    "v_mfma_f32_16x16x32_bf16 v[40:43], a[44:47], v[100:103], v[40:43]\n\t"
    "s_waitcnt lgkmcnt(8)\n\t"
    "v_cndmask_b32_dpp v126, v20, v22, vcc quad_perm:[1,0,3,2] row_mask:0xf bank_mask:0xf\n\t"
    "v_cndmask_b32_dpp v127, v21, v23, vcc quad_perm:[1,0,3,2] row_mask:0xf bank_mask:0xf\n\t"
    "v_mfma_f32_16x16x32_bf16 v[40:43], a[48:51], v[104:107], v[40:43]\n\t"
    "s_waitcnt lgkmcnt(1)\n\t"
    "s_mov_b64 vcc, s[46:47]\n\t"
    "v_cndmask_b32_dpp v124, v22, v20, vcc quad_perm:[1,0,3,2] row_mask:0xf bank_mask:0xf\n\t"
    "v_mfma_f32_16x16x32_bf16 v[40:43], a[52:55], v[108:111], v[40:43]\n\t"
    "v_cndmask_b32_dpp v125, v23, v21, vcc quad_perm:[1,0,3,2] row_mask:0xf bank_mask:0xf\n\t"
    "v_permlane16_swap_b32 v124, v126\n\t"
    "v_mfma_f32_16x16x32_bf16 v[44:47], a[84:87], v[84:87], v[44:47]\n\t"
    "v_permlane16_swap_b32 v125, v127\n\t"
    "ds_read_b64_tr_b16 a[110:111], v6 offset:2304\n\t"
    "v_mfma_f32_16x16x32_bf16 v[44:47], a[88:91], v[88:91], v[44:47]\n\t"
    "buffer_store_dwordx4 v[248:251], v17, s[24:27], s66 offen offset:512 nt\n\t"
    "v_mfma_f32_16x16x32_bf16 v[44:47], a[92:95], v[92:95], v[44:47]\n\t"
    "v_mfma_f32_16x16x32_bf16 v[44:47], a[96:99], v[96:99], v[44:47]\n\t"
    "v_mfma_f32_16x16x32_bf16 v[44:47], a[100:103], v[100:103], v[44:47]\n\t"
    "v_mfma_f32_16x16x32_bf16 v[44:47], a[104:107], v[104:107], v[44:47]\n\t"
    "s_waitcnt lgkmcnt(0)\n\t"
    "v_mfma_f32_16x16x32_bf16 v[44:47], a[108:111], v[108:111], v[44:47]\n\t"
    "v_mfma_f32_16x16x32_bf16 v[48:51], a[112:115], v[56:59], 0\n\t"
    "ds_read_b128 v[84:87], v9 offset:31744\n\t"
    "v_mfma_f32_16x16x32_bf16 v[48:51], a[116:119], v[60:63], v[48:51]\n\t"
    "v_mfma_f32_16x16x32_bf16 v[48:51], a[120:123], v[64:67], v[48:51]\n\t"
    "v_cvt_pk_bf16_f32 v20, v40, v41\n\t"
    "v_cvt_pk_bf16_f32 v21, v42, v43\n\t"
    "v_mfma_f32_16x16x32_bf16 v[48:51], a[124:127], v[68:71], v[48:51]\n\t"
    "buffer_store_dwordx4 v[112:115], v17, s[24:27], s68 offen offset:256 nt\n\t"
    "v_mfma_f32_16x16x32_bf16 v[48:51], a[128:131], v[72:75], v[48:51]\n\t"
    "ds_read_b128 v[88:91], v8 offset:32768\n\t"
    "v_cvt_pk_bf16_f32 v22, v44, v45\n\t"
    "v_mfma_f32_16x16x32_bf16 v[48:51], a[132:135], v[76:79], v[48:51]\n\t"
    "v_cvt_pk_bf16_f32 v23, v46, v47\n\t"
    "v_cndmask_b32_dpp v128, v22, v20, vcc quad_perm:[1,0,3,2] row_mask:0xf bank_mask:0xf\n\t"
    "v_mfma_f32_16x16x32_bf16 v[48:51], a[136:139], v[80:83], v[48:51]\n\t"
    "v_cndmask_b32_dpp v129, v23, v21, vcc quad_perm:[1,0,3,2] row_mask:0xf bank_mask:0xf\n\t"
    "s_mov_b64 vcc, s[34:35]\n\t"
    "v_mfma_f32_16x16x32_bf16 v[52:55], a[168:171], v[56:59], 0\n\t"
    "v_cndmask_b32_dpp v130, v20, v22, vcc quad_perm:[1,0,3,2] row_mask:0xf bank_mask:0xf\n\t"
    "v_cndmask_b32_dpp v131, v21, v23, vcc quad_perm:[1,0,3,2] row_mask:0xf bank_mask:0xf\n\t"
    "v_mfma_f32_16x16x32_bf16 v[52:55], a[172:175], v[60:63], v[52:55]\n\t"
    "ds_read_b128 v[92:95], v9 offset:32768\n\t"
    "v_permlane16_swap_b32 v128, v130\n\t"
    "v_mfma_f32_16x16x32_bf16 v[52:55], a[176:179], v[64:67], v[52:55]\n\t"
    "v_permlane16_swap_b32 v129, v131\n\t"
    "v_mfma_f32_16x16x32_bf16 v[52:55], a[180:183], v[68:71], v[52:55]\n\t"
    "v_mfma_f32_16x16x32_bf16 v[52:55], a[184:187], v[72:75], v[52:55]\n\t"
    "v_mfma_f32_16x16x32_bf16 v[52:55], a[188:191], v[76:79], v[52:55]\n\t"
    "ds_read_b128 v[96:99], v8 offset:33792\n\t"
    "v_mfma_f32_16x16x32_bf16 v[52:55], a[192:195], v[80:83], v[52:55]\n\t"
    "buffer_store_dwordx4 v[116:119], v17, s[24:27], s68 offen offset:512 nt\n\t"
    "v_mfma_f32_16x16x32_bf16 v[24:27], v[136:139], v[56:59], 0\n\t"
    "v_mfma_f32_16x16x32_bf16 v[24:27], v[140:143], v[60:63], v[24:27]\n\t"
    "v_mfma_f32_16x16x32_bf16 v[24:27], v[144:147], v[64:67], v[24:27]\n\t"
    "v_mfma_f32_16x16x32_bf16 v[24:27], v[148:151], v[68:71], v[24:27]\n\t"
    "ds_read_b128 v[100:103], v9 offset:33792\n\t"
    "v_mfma_f32_16x16x32_bf16 v[24:27], v[152:155], v[72:75], v[24:27]\n\t"
    "v_mfma_f32_16x16x32_bf16 v[24:27], v[156:159], v[76:79], v[24:27]\n\t"
    "v_mfma_f32_16x16x32_bf16 v[24:27], v[160:163], v[80:83], v[24:27]\n\t"
    "v_mfma_f32_16x16x32_bf16 v[28:31], v[192:195], v[56:59], 0\n\t"
    "ds_read_b128 v[104:107], v8 offset:34816\n\t"
    "v_mfma_f32_16x16x32_bf16 v[28:31], v[196:199], v[60:63], v[28:31]\n\t"
    "buffer_store_dwordx4 v[120:123], v16, s[24:27], s64 offen offset:256 nt\n\t"
    "v_mfma_f32_16x16x32_bf16 v[28:31], v[200:203], v[64:67], v[28:31]\n\t"
    "v_mfma_f32_16x16x32_bf16 v[28:31], v[204:207], v[68:71], v[28:31]\n\t"
    "v_mfma_f32_16x16x32_bf16 v[28:31], v[208:211], v[72:75], v[28:31]\n\t"
    "ds_read_b128 v[108:111], v9 offset:34816\n\t"
    "v_mfma_f32_16x16x32_bf16 v[28:31], v[212:215], v[76:79], v[28:31]\n\t"
    "v_mfma_f32_16x16x32_bf16 v[28:31], v[216:219], v[80:83], v[28:31]\n\t"
    "v_mfma_f32_16x16x32_bf16 v[32:35], a[0:3], v[56:59], 0\n\t"
    "v_mfma_f32_16x16x32_bf16 v[32:35], a[4:7], v[60:63], v[32:35]\n\t"
    "v_mfma_f32_16x16x32_bf16 v[32:35], a[8:11], v[64:67], v[32:35]\n\t"
    "buffer_store_dwordx4 v[124:127], v16, s[24:27], s64 offen offset:512 nt\n\t"
    "v_mfma_f32_16x16x32_bf16 v[32:35], a[12:15], v[68:71], v[32:35]\n\t"
    "v_mfma_f32_16x16x32_bf16 v[32:35], a[16:19], v[72:75], v[32:35]\n\t"
    "v_mfma_f32_16x16x32_bf16 v[32:35], a[20:23], v[76:79], v[32:35]\n\t"
    "v_mfma_f32_16x16x32_bf16 v[32:35], a[24:27], v[80:83], v[32:35]\n\t"
    "v_mfma_f32_16x16x32_bf16 v[36:39], a[56:59], v[56:59], 0\n\t"
    "v_mfma_f32_16x16x32_bf16 v[36:39], a[60:63], v[60:63], v[36:39]\n\t"
    "v_mfma_f32_16x16x32_bf16 v[36:39], a[64:67], v[64:67], v[36:39]\n\t"
    "v_mfma_f32_16x16x32_bf16 v[36:39], a[68:71], v[68:71], v[36:39]\n\t"
    "v_mfma_f32_16x16x32_bf16 v[36:39], a[72:75], v[72:75], v[36:39]\n\t"
    "buffer_store_dwordx4 v[128:131], v18, s[24:27], s64 offen nt\n\t"
    "v_mfma_f32_16x16x32_bf16 v[36:39], a[76:79], v[76:79], v[36:39]\n\t"
    "v_mfma_f32_16x16x32_bf16 v[36:39], a[80:83], v[80:83], v[36:39]\n\t"
    "s_waitcnt lgkmcnt(5)\n\t"
    "v_mfma_f32_16x16x32_bf16 v[48:51], a[140:143], v[84:87], v[48:51]\n\t"
    "ds_read_b128 v[56:59], v12 offset:0\n\t"
    "v_mfma_f32_16x16x32_bf16 v[48:51], a[144:147], v[88:91], v[48:51]\n\t"
    "s_waitcnt lgkmcnt(4)\n\t"
    "v_mfma_f32_16x16x32_bf16 v[48:51], a[148:151], v[92:95], v[48:51]\n\t"
    "v_mfma_f32_16x16x32_bf16 v[48:51], a[152:155], v[96:99], v[48:51]\n\t"
    "s_waitcnt lgkmcnt(2)\n\t"
    "v_mfma_f32_16x16x32_bf16 v[48:51], a[156:159], v[100:103], v[48:51]\n\t"
    "ds_read_b128 v[60:63], v13 offset:0\n\t"
    "v_mfma_f32_16x16x32_bf16 v[48:51], a[160:163], v[104:107], v[48:51]\n\t"
    "s_waitcnt lgkmcnt(2)\n\t"
    "v_mfma_f32_16x16x32_bf16 v[48:51], a[164:167], v[108:111], v[48:51]\n\t"
    "v_mfma_f32_16x16x32_bf16 v[52:55], a[196:199], v[84:87], v[52:55]\n\t"
    "v_mfma_f32_16x16x32_bf16 v[52:55], a[200:203], v[88:91], v[52:55]\n\t"
    "ds_read_b128 v[64:67], v12 offset:1024\n\t"
    "v_mfma_f32_16x16x32_bf16 v[52:55], a[204:207], v[92:95], v[52:55]\n\t"
    "v_mfma_f32_16x16x32_bf16 v[52:55], a[208:211], v[96:99], v[52:55]\n\t"
    "v_mfma_f32_16x16x32_bf16 v[52:55], a[212:215], v[100:103], v[52:55]\n\t"
    "v_mfma_f32_16x16x32_bf16 v[52:55], a[216:219], v[104:107], v[52:55]\n\t"
    "ds_read_b128 v[68:71], v13 offset:1024\n\t"
    "v_mfma_f32_16x16x32_bf16 v[52:55], a[220:223], v[108:111], v[52:55]\n\t"
    "v_mfma_f32_16x16x32_bf16 v[24:27], v[164:167], v[84:87], v[24:27]\n\t"
    "v_mfma_f32_16x16x32_bf16 v[24:27], v[168:171], v[88:91], v[24:27]\n\t"
    "v_mfma_f32_16x16x32_bf16 v[24:27], v[172:175], v[92:95], v[24:27]\n\t"
    "v_cvt_pk_bf16_f32 v20, v48, v49\n\t"
    "v_cvt_pk_bf16_f32 v21, v50, v51\n\t"
    "v_mfma_f32_16x16x32_bf16 v[24:27], v[176:179], v[96:99], v[24:27]\n\t"
    "ds_read_b128 v[72:75], v12 offset:2048\n\t"
    "v_mfma_f32_16x16x32_bf16 v[24:27], v[180:183], v[100:103], v[24:27]\n\t"
    "v_cvt_pk_bf16_f32 v22, v52, v53\n\t"
    "v_cvt_pk_bf16_f32 v23, v54, v55\n\t"
    "v_mfma_f32_16x16x32_bf16 v[24:27], v[184:187], v[104:107], v[24:27]\n\t"
    "v_cndmask_b32_dpp v134, v20, v22, vcc quad_perm:[1,0,3,2] row_mask:0xf bank_mask:0xf\n\t"
    "v_cndmask_b32_dpp v135, v21, v23, vcc quad_perm:[1,0,3,2] row_mask:0xf bank_mask:0xf\n\t"
    "v_mfma_f32_16x16x32_bf16 v[24:27], v[188:191], v[108:111], v[24:27]\n\t"
    "s_mov_b64 vcc, s[46:47]\n\t"
    "v_cndmask_b32_dpp v132, v22, v20, vcc quad_perm:[1,0,3,2] row_mask:0xf bank_mask:0xf\n\t"
    "v_cndmask_b32_dpp v133, v23, v21, vcc quad_perm:[1,0,3,2] row_mask:0xf bank_mask:0xf\n\t"
    "v_mfma_f32_16x16x32_bf16 v[28:31], v[220:223], v[84:87], v[28:31]\n\t"
    "ds_read_b128 v[76:79], v13 offset:2048\n\t"
    "v_permlane16_swap_b32 v132, v134\n\t"
    "v_mfma_f32_16x16x32_bf16 v[28:31], v[224:227], v[88:91], v[28:31]\n\t"
    "v_permlane16_swap_b32 v133, v135\n\t"
    "v_mfma_f32_16x16x32_bf16 v[28:31], v[228:231], v[92:95], v[28:31]\n\t"
    "buffer_store_dwordx4 v[132:135], v16, s[24:27], s66 offen offset:256 nt\n\t"
    "v_mfma_f32_16x16x32_bf16 v[28:31], v[232:235], v[96:99], v[28:31]\n\t"
    "v_mfma_f32_16x16x32_bf16 v[28:31], v[236:239], v[100:103], v[28:31]\n\t"
    "ds_read_b128 v[80:83], v12 offset:3072\n\t"
    "v_mfma_f32_16x16x32_bf16 v[28:31], v[240:243], v[104:107], v[28:31]\n\t"
    "v_mfma_f32_16x16x32_bf16 v[28:31], v[244:247], v[108:111], v[28:31]\n\t"
    "v_mfma_f32_16x16x32_bf16 v[32:35], a[28:31], v[84:87], v[32:35]\n\t"
    "v_mfma_f32_16x16x32_bf16 v[32:35], a[32:35], v[88:91], v[32:35]\n\t"
    "v_mfma_f32_16x16x32_bf16 v[32:35], a[36:39], v[92:95], v[32:35]\n\t"
    "v_cvt_pk_bf16_f32 v20, v24, v25\n\t"
    "v_cvt_pk_bf16_f32 v21, v26, v27\n\t"
    "v_mfma_f32_16x16x32_bf16 v[32:35], a[40:43], v[96:99], v[32:35]\n\t"
    "v_mfma_f32_16x16x32_bf16 v[32:35], a[44:47], v[100:103], v[32:35]\n\t"
    "v_mfma_f32_16x16x32_bf16 v[32:35], a[48:51], v[104:107], v[32:35]\n\t"
    "v_cvt_pk_bf16_f32 v22, v28, v29\n\t"
    "v_cvt_pk_bf16_f32 v23, v30, v31\n\t"
    "v_mfma_f32_16x16x32_bf16 v[32:35], a[52:55], v[108:111], v[32:35]\n\t"
    "v_cndmask_b32_dpp v248, v22, v20, vcc quad_perm:[1,0,3,2] row_mask:0xf bank_mask:0xf\n\t"
    "v_cndmask_b32_dpp v249, v23, v21, vcc quad_perm:[1,0,3,2] row_mask:0xf bank_mask:0xf\n\t"
    "v_mfma_f32_16x16x32_bf16 v[36:39], a[84:87], v[84:87], v[36:39]\n\t"
    "s_mov_b64 vcc, s[34:35]\n\t"
    "v_cndmask_b32_dpp v250, v20, v22, vcc quad_perm:[1,0,3,2] row_mask:0xf bank_mask:0xf\n\t"
    "v_cndmask_b32_dpp v251, v21, v23, vcc quad_perm:[1,0,3,2] row_mask:0xf bank_mask:0xf\n\t"
    "v_mfma_f32_16x16x32_bf16 v[36:39], a[88:91], v[88:91], v[36:39]\n\t"
    "v_permlane16_swap_b32 v248, v250\n\t"
    "v_permlane16_swap_b32 v249, v251\n\t"
    "v_mfma_f32_16x16x32_bf16 v[36:39], a[92:95], v[92:95], v[36:39]\n\t"
    "buffer_store_dwordx4 v[248:251], v16, s[24:27], s66 offen offset:512 nt\n\t"
    "v_mfma_f32_16x16x32_bf16 v[36:39], a[96:99], v[96:99], v[36:39]\n\t"
    "v_mfma_f32_16x16x32_bf16 v[36:39], a[100:103], v[100:103], v[36:39]\n\t"
    "v_mfma_f32_16x16x32_bf16 v[36:39], a[104:107], v[104:107], v[36:39]\n\t"
    "v_mfma_f32_16x16x32_bf16 v[36:39], a[108:111], v[108:111], v[36:39]\n\t"
    "s_waitcnt lgkmcnt(5)\n\t"
    "v_mfma_f32_16x16x32_bf16 v[40:43], a[112:115], v[56:59], 0\n\t"
    "ds_read_b128 v[84:87], v13 offset:3072\n\t"
    "v_mfma_f32_16x16x32_bf16 v[40:43], a[116:119], v[60:63], v[40:43]\n\t"
    "s_waitcnt lgkmcnt(4)\n\t"
    "v_mfma_f32_16x16x32_bf16 v[40:43], a[120:123], v[64:67], v[40:43]\n\t"
    "v_cvt_pk_bf16_f32 v20, v32, v33\n\t"
    "v_cvt_pk_bf16_f32 v21, v34, v35\n\t"
    "v_mfma_f32_16x16x32_bf16 v[40:43], a[124:127], v[68:71], v[40:43]\n\t"
    "s_waitcnt lgkmcnt(2)\n\t"
    "v_cvt_pk_bf16_f32 v22, v36, v37\n\t"
    "v_cvt_pk_bf16_f32 v23, v38, v39\n\t"
    "v_mfma_f32_16x16x32_bf16 v[40:43], a[128:131], v[72:75], v[40:43]\n\t"
    "ds_read_b128 v[88:91], v12 offset:4096\n\t"
    "v_cndmask_b32_dpp v114, v20, v22, vcc quad_perm:[1,0,3,2] row_mask:0xf bank_mask:0xf\n\t"
    "v_mfma_f32_16x16x32_bf16 v[40:43], a[132:135], v[76:79], v[40:43]\n\t"
    "s_waitcnt lgkmcnt(2)\n\t"
    "v_cndmask_b32_dpp v115, v21, v23, vcc quad_perm:[1,0,3,2] row_mask:0xf bank_mask:0xf\n\t"
    "s_mov_b64 vcc, s[46:47]\n\t"
    "v_mfma_f32_16x16x32_bf16 v[40:43], a[136:139], v[80:83], v[40:43]\n\t"
    "v_cndmask_b32_dpp v112, v22, v20, vcc quad_perm:[1,0,3,2] row_mask:0xf bank_mask:0xf\n\t"
    "v_cndmask_b32_dpp v113, v23, v21, vcc quad_perm:[1,0,3,2] row_mask:0xf bank_mask:0xf\n\t"
    "v_mfma_f32_16x16x32_bf16 v[44:47], a[168:171], v[56:59], 0\n\t"
    "v_permlane16_swap_b32 v112, v114\n\t"
    "v_permlane16_swap_b32 v113, v115\n\t"
    "v_mfma_f32_16x16x32_bf16 v[44:47], a[172:175], v[60:63], v[44:47]\n\t"
    "ds_read_b128 v[92:95], v13 offset:4096\n\t"
    "v_mfma_f32_16x16x32_bf16 v[44:47], a[176:179], v[64:67], v[44:47]\n\t"
    "buffer_store_dwordx4 v[112:115], v18, s[24:27], s66 offen nt\n\t"
    "v_mfma_f32_16x16x32_bf16 v[44:47], a[180:183], v[68:71], v[44:47]\n\t"
    "v_mfma_f32_16x16x32_bf16 v[44:47], a[184:187], v[72:75], v[44:47]\n\t"
    "v_mfma_f32_16x16x32_bf16 v[44:47], a[188:191], v[76:79], v[44:47]\n\t"
    "ds_read_b128 v[96:99], v12 offset:5120\n\t"
    "v_mfma_f32_16x16x32_bf16 v[44:47], a[192:195], v[80:83], v[44:47]\n\t"
    "v_mfma_f32_16x16x32_bf16 v[48:51], v[136:139], v[56:59], 0\n\t"
    "v_mfma_f32_16x16x32_bf16 v[48:51], v[140:143], v[60:63], v[48:51]\n\t"
    "v_mfma_f32_16x16x32_bf16 v[48:51], v[144:147], v[64:67], v[48:51]\n\t"
    "v_mfma_f32_16x16x32_bf16 v[48:51], v[148:151], v[68:71], v[48:51]\n\t"
    "ds_read_b128 v[100:103], v13 offset:5120\n\t"
    "v_mfma_f32_16x16x32_bf16 v[48:51], v[152:155], v[72:75], v[48:51]\n\t"
    "v_mfma_f32_16x16x32_bf16 v[48:51], v[156:159], v[76:79], v[48:51]\n\t"
    "v_mfma_f32_16x16x32_bf16 v[48:51], v[160:163], v[80:83], v[48:51]\n\t"
    "v_mfma_f32_16x16x32_bf16 v[52:55], v[192:195], v[56:59], 0\n\t"
    "ds_read_b128 v[104:107], v12 offset:6144\n\t"
    "v_mfma_f32_16x16x32_bf16 v[52:55], v[196:199], v[60:63], v[52:55]\n\t"
    "v_mfma_f32_16x16x32_bf16 v[52:55], v[200:203], v[64:67], v[52:55]\n\t"
    "v_mfma_f32_16x16x32_bf16 v[52:55], v[204:207], v[68:71], v[52:55]\n\t"
    "v_mfma_f32_16x16x32_bf16 v[52:55], v[208:211], v[72:75], v[52:55]\n\t"
    "ds_read_b128 v[108:111], v13 offset:6144\n\t"
    "v_mfma_f32_16x16x32_bf16 v[52:55], v[212:215], v[76:79], v[52:55]\n\t"
    "v_mfma_f32_16x16x32_bf16 v[52:55], v[216:219], v[80:83], v[52:55]\n\t"
    "v_mfma_f32_16x16x32_bf16 v[24:27], a[0:3], v[56:59], 0\n\t"
    "v_mfma_f32_16x16x32_bf16 v[24:27], a[4:7], v[60:63], v[24:27]\n\t"
    "v_mfma_f32_16x16x32_bf16 v[24:27], a[8:11], v[64:67], v[24:27]\n\t"
    "v_mfma_f32_16x16x32_bf16 v[24:27], a[12:15], v[68:71], v[24:27]\n\t"
    "v_mfma_f32_16x16x32_bf16 v[24:27], a[16:19], v[72:75], v[24:27]\n\t"
    "v_mfma_f32_16x16x32_bf16 v[24:27], a[20:23], v[76:79], v[24:27]\n\t"
    "v_mfma_f32_16x16x32_bf16 v[24:27], a[24:27], v[80:83], v[24:27]\n\t"
    "v_mfma_f32_16x16x32_bf16 v[28:31], a[56:59], v[56:59], 0\n\t"
    "v_mfma_f32_16x16x32_bf16 v[28:31], a[60:63], v[60:63], v[28:31]\n\t"
    "v_mfma_f32_16x16x32_bf16 v[28:31], a[64:67], v[64:67], v[28:31]\n\t"
    "v_mfma_f32_16x16x32_bf16 v[28:31], a[68:71], v[68:71], v[28:31]\n\t"
    "v_mfma_f32_16x16x32_bf16 v[28:31], a[72:75], v[72:75], v[28:31]\n\t"
    "v_mfma_f32_16x16x32_bf16 v[28:31], a[76:79], v[76:79], v[28:31]\n\t"
    "v_mfma_f32_16x16x32_bf16 v[28:31], a[80:83], v[80:83], v[28:31]\n\t"
    "s_waitcnt lgkmcnt(5)\n\t"
    "v_mfma_f32_16x16x32_bf16 v[40:43], a[140:143], v[84:87], v[40:43]\n\t"
    "ds_read_b128 v[56:59], v14 offset:28672\n\t"
    "v_mfma_f32_16x16x32_bf16 v[40:43], a[144:147], v[88:91], v[40:43]\n\t"
    "s_waitcnt lgkmcnt(4)\n\t"
    "v_mfma_f32_16x16x32_bf16 v[40:43], a[148:151], v[92:95], v[40:43]\n\t"
    "v_mfma_f32_16x16x32_bf16 v[40:43], a[152:155], v[96:99], v[40:43]\n\t"
    "s_waitcnt lgkmcnt(2)\n\t"
    "v_mfma_f32_16x16x32_bf16 v[40:43], a[156:159], v[100:103], v[40:43]\n\t"
    "ds_read_b128 v[60:63], v15 offset:28672\n\t"
    "v_mfma_f32_16x16x32_bf16 v[40:43], a[160:163], v[104:107], v[40:43]\n\t"
    "s_waitcnt lgkmcnt(2)\n\t"
    "v_mfma_f32_16x16x32_bf16 v[40:43], a[164:167], v[108:111], v[40:43]\n\t"
    "v_mfma_f32_16x16x32_bf16 v[44:47], a[196:199], v[84:87], v[44:47]\n\t"
    "v_mfma_f32_16x16x32_bf16 v[44:47], a[200:203], v[88:91], v[44:47]\n\t"
    "ds_read_b128 v[64:67], v14 offset:29696\n\t"
    "v_mfma_f32_16x16x32_bf16 v[44:47], a[204:207], v[92:95], v[44:47]\n\t"
    "v_mfma_f32_16x16x32_bf16 v[44:47], a[208:211], v[96:99], v[44:47]\n\t"
    "v_mfma_f32_16x16x32_bf16 v[44:47], a[212:215], v[100:103], v[44:47]\n\t"
    "v_mfma_f32_16x16x32_bf16 v[44:47], a[216:219], v[104:107], v[44:47]\n\t"
    "ds_read_b128 v[68:71], v15 offset:29696\n\t"
    "v_mfma_f32_16x16x32_bf16 v[44:47], a[220:223], v[108:111], v[44:47]\n\t"
    "v_mfma_f32_16x16x32_bf16 v[48:51], v[164:167], v[84:87], v[48:51]\n\t"
    "v_mfma_f32_16x16x32_bf16 v[48:51], v[168:171], v[88:91], v[48:51]\n\t"
    "v_mfma_f32_16x16x32_bf16 v[48:51], v[172:175], v[92:95], v[48:51]\n\t"
    "v_cvt_pk_bf16_f32 v20, v40, v41\n\t"
    "v_cvt_pk_bf16_f32 v21, v42, v43\n\t"
    "v_mfma_f32_16x16x32_bf16 v[48:51], v[176:179], v[96:99], v[48:51]\n\t"
    "ds_read_b128 v[72:75], v14 offset:30720\n\t"
    "v_mfma_f32_16x16x32_bf16 v[48:51], v[180:183], v[100:103], v[48:51]\n\t"
    "v_cvt_pk_bf16_f32 v22, v44, v45\n\t"
    "v_cvt_pk_bf16_f32 v23, v46, v47\n\t"
    "v_mfma_f32_16x16x32_bf16 v[48:51], v[184:187], v[104:107], v[48:51]\n\t"
    "v_cndmask_b32_dpp v116, v22, v20, vcc quad_perm:[1,0,3,2] row_mask:0xf bank_mask:0xf\n\t"
    "v_cndmask_b32_dpp v117, v23, v21, vcc quad_perm:[1,0,3,2] row_mask:0xf bank_mask:0xf\n\t"
    "v_mfma_f32_16x16x32_bf16 v[48:51], v[188:191], v[108:111], v[48:51]\n\t"
    "s_mov_b64 vcc, s[34:35]\n\t"
    "v_cndmask_b32_dpp v118, v20, v22, vcc quad_perm:[1,0,3,2] row_mask:0xf bank_mask:0xf\n\t"
    "v_cndmask_b32_dpp v119, v21, v23, vcc quad_perm:[1,0,3,2] row_mask:0xf bank_mask:0xf\n\t"
    "v_mfma_f32_16x16x32_bf16 v[52:55], v[220:223], v[84:87], v[52:55]\n\t"
    "ds_read_b128 v[76:79], v15 offset:30720\n\t"
    "v_permlane16_swap_b32 v116, v118\n\t"
    "v_mfma_f32_16x16x32_bf16 v[52:55], v[224:227], v[88:91], v[52:55]\n\t"
    "v_permlane16_swap_b32 v117, v119\n\t"
    "v_mfma_f32_16x16x32_bf16 v[52:55], v[228:231], v[92:95], v[52:55]\n\t"
    "buffer_store_dwordx4 v[116:119], v16, s[24:27], s68 offen offset:256 nt\n\t"
    "v_mfma_f32_16x16x32_bf16 v[52:55], v[232:235], v[96:99], v[52:55]\n\t"
    "v_mfma_f32_16x16x32_bf16 v[52:55], v[236:239], v[100:103], v[52:55]\n\t"
    "ds_read_b128 v[80:83], v14 offset:31744\n\t"
    "v_mfma_f32_16x16x32_bf16 v[52:55], v[240:243], v[104:107], v[52:55]\n\t"
    "v_mfma_f32_16x16x32_bf16 v[52:55], v[244:247], v[108:111], v[52:55]\n\t"
    "v_mfma_f32_16x16x32_bf16 v[24:27], a[28:31], v[84:87], v[24:27]\n\t"
    "v_mfma_f32_16x16x32_bf16 v[24:27], a[32:35], v[88:91], v[24:27]\n\t"
    "v_mfma_f32_16x16x32_bf16 v[24:27], a[36:39], v[92:95], v[24:27]\n\t"
    "v_cvt_pk_bf16_f32 v20, v48, v49\n\t"
    "v_cvt_pk_bf16_f32 v21, v50, v51\n\t"
    "v_mfma_f32_16x16x32_bf16 v[24:27], a[40:43], v[96:99], v[24:27]\n\t"
    "v_mfma_f32_16x16x32_bf16 v[24:27], a[44:47], v[100:103], v[24:27]\n\t"
    "v_mfma_f32_16x16x32_bf16 v[24:27], a[48:51], v[104:107], v[24:27]\n\t"
    "v_cvt_pk_bf16_f32 v22, v52, v53\n\t"
    "v_cvt_pk_bf16_f32 v23, v54, v55\n\t"
    "v_mfma_f32_16x16x32_bf16 v[24:27], a[52:55], v[108:111], v[24:27]\n\t"
    "v_cndmask_b32_dpp v122, v20, v22, vcc quad_perm:[1,0,3,2] row_mask:0xf bank_mask:0xf\n\t"
    "v_cndmask_b32_dpp v123, v21, v23, vcc quad_perm:[1,0,3,2] row_mask:0xf bank_mask:0xf\n\t"
    "v_mfma_f32_16x16x32_bf16 v[28:31], a[84:87], v[84:87], v[28:31]\n\t"
    "s_mov_b64 vcc, s[46:47]\n\t"
    "v_cndmask_b32_dpp v120, v22, v20, vcc quad_perm:[1,0,3,2] row_mask:0xf bank_mask:0xf\n\t"
    "v_cndmask_b32_dpp v121, v23, v21, vcc quad_perm:[1,0,3,2] row_mask:0xf bank_mask:0xf\n\t"
    "v_mfma_f32_16x16x32_bf16 v[28:31], a[88:91], v[88:91], v[28:31]\n\t"
    "v_permlane16_swap_b32 v120, v122\n\t"
    "v_permlane16_swap_b32 v121, v123\n\t"
    "v_mfma_f32_16x16x32_bf16 v[28:31], a[92:95], v[92:95], v[28:31]\n\t"
    "buffer_store_dwordx4 v[120:123], v16, s[24:27], s68 offen offset:512 nt\n\t"
    "v_mfma_f32_16x16x32_bf16 v[28:31], a[96:99], v[96:99], v[28:31]\n\t"
    "v_mfma_f32_16x16x32_bf16 v[28:31], a[100:103], v[100:103], v[28:31]\n\t"
    "v_mfma_f32_16x16x32_bf16 v[28:31], a[104:107], v[104:107], v[28:31]\n\t"
    "v_mfma_f32_16x16x32_bf16 v[28:31], a[108:111], v[108:111], v[28:31]\n\t"
    "s_waitcnt lgkmcnt(5)\n\t"
    "v_mfma_f32_16x16x32_bf16 v[32:35], v[136:139], v[56:59], 0\n\t"
    "ds_read_b128 v[84:87], v15 offset:31744\n\t"
    "v_mfma_f32_16x16x32_bf16 v[32:35], v[140:143], v[60:63], v[32:35]\n\t"
    "s_waitcnt lgkmcnt(4)\n\t"
    "ds_read_b128 v[88:91], v14 offset:32768\n\t"
    "v_mfma_f32_16x16x32_bf16 v[32:35], v[144:147], v[64:67], v[32:35]\n\t"
    "v_cvt_pk_bf16_f32 v20, v24, v25\n\t"
    "v_cvt_pk_bf16_f32 v21, v26, v27\n\t"
    "v_mfma_f32_16x16x32_bf16 v[32:35], v[148:151], v[68:71], v[32:35]\n\t"
    "s_waitcnt lgkmcnt(3)\n\t"
    "ds_read_b128 v[92:95], v15 offset:32768\n\t"
    "v_cvt_pk_bf16_f32 v22, v28, v29\n\t"
    "v_mfma_f32_16x16x32_bf16 v[32:35], v[152:155], v[72:75], v[32:35]\n\t"
    "ds_read_b128 v[96:99], v14 offset:33792\n\t"
    "v_cvt_pk_bf16_f32 v23, v30, v31\n\t"
    "v_mfma_f32_16x16x32_bf16 v[32:35], v[156:159], v[76:79], v[32:35]\n\t"
    "s_waitcnt lgkmcnt(4)\n\t"
    "v_cndmask_b32_dpp v124, v22, v20, vcc quad_perm:[1,0,3,2] row_mask:0xf bank_mask:0xf\n\t"
    "v_cndmask_b32_dpp v125, v23, v21, vcc quad_perm:[1,0,3,2] row_mask:0xf bank_mask:0xf\n\t"
    "v_mfma_f32_16x16x32_bf16 v[32:35], v[160:163], v[80:83], v[32:35]\n\t"
    "s_mov_b64 vcc, s[34:35]\n\t"
    "ds_read_b128 v[100:103], v15 offset:33792\n\t"
    "v_cndmask_b32_dpp v126, v20, v22, vcc quad_perm:[1,0,3,2] row_mask:0xf bank_mask:0xf\n\t"
    "v_mfma_f32_16x16x32_bf16 v[36:39], v[192:195], v[56:59], 0\n\t"
    "ds_read_b128 v[104:107], v14 offset:34816\n\t"
    "v_cndmask_b32_dpp v127, v21, v23, vcc quad_perm:[1,0,3,2] row_mask:0xf bank_mask:0xf\n\t"
    "v_mfma_f32_16x16x32_bf16 v[36:39], v[196:199], v[60:63], v[36:39]\n\t"
    "v_permlane16_swap_b32 v124, v126\n\t"
    "v_permlane16_swap_b32 v125, v127\n\t"
    "v_mfma_f32_16x16x32_bf16 v[36:39], v[200:203], v[64:67], v[36:39]\n\t"
    "ds_read_b128 v[108:111], v15 offset:34816\n\t"
    "v_mfma_f32_16x16x32_bf16 v[36:39], v[204:207], v[68:71], v[36:39]\n\t"
    "buffer_store_dwordx4 v[124:127], v18, s[24:27], s68 offen nt\n\t"
    "v_mfma_f32_16x16x32_bf16 v[36:39], v[208:211], v[72:75], v[36:39]\n\t"
    "v_mfma_f32_16x16x32_bf16 v[36:39], v[212:215], v[76:79], v[36:39]\n\t"
    "v_mfma_f32_16x16x32_bf16 v[36:39], v[216:219], v[80:83], v[36:39]\n\t"
    "s_waitcnt lgkmcnt(5)\n\t"
    "v_mfma_f32_16x16x32_bf16 v[32:35], v[164:167], v[84:87], v[32:35]\n\t"
    "ds_read_b128 v[56:59], v12 offset:28672\n\t"
    "v_mfma_f32_16x16x32_bf16 v[32:35], v[168:171], v[88:91], v[32:35]\n\t"
    "s_waitcnt lgkmcnt(4)\n\t"
    "ds_read_b128 v[60:63], v13 offset:28672\n\t"
    "v_mfma_f32_16x16x32_bf16 v[32:35], v[172:175], v[92:95], v[32:35]\n\t"
    "v_mfma_f32_16x16x32_bf16 v[32:35], v[176:179], v[96:99], v[32:35]\n\t"
    "s_waitcnt lgkmcnt(3)\n\t"
    "ds_read_b128 v[64:67], v12 offset:29696\n\t"
    "v_mfma_f32_16x16x32_bf16 v[32:35], v[180:183], v[100:103], v[32:35]\n\t"
    "ds_read_b128 v[68:71], v13 offset:29696\n\t"
    "v_mfma_f32_16x16x32_bf16 v[32:35], v[184:187], v[104:107], v[32:35]\n\t"
    "s_waitcnt lgkmcnt(4)\n\t"
    "v_mfma_f32_16x16x32_bf16 v[32:35], v[188:191], v[108:111], v[32:35]\n\t"
    "ds_read_b128 v[72:75], v12 offset:30720\n\t"
    "v_mfma_f32_16x16x32_bf16 v[36:39], v[220:223], v[84:87], v[36:39]\n\t"
    "ds_read_b128 v[76:79], v13 offset:30720\n\t"
    "v_mfma_f32_16x16x32_bf16 v[36:39], v[224:227], v[88:91], v[36:39]\n\t"
    "v_mfma_f32_16x16x32_bf16 v[36:39], v[228:231], v[92:95], v[36:39]\n\t"
    "ds_read_b128 v[80:83], v12 offset:31744\n\t"
    "v_mfma_f32_16x16x32_bf16 v[36:39], v[232:235], v[96:99], v[36:39]\n\t"
    "v_mfma_f32_16x16x32_bf16 v[36:39], v[236:239], v[100:103], v[36:39]\n\t"
    "v_mfma_f32_16x16x32_bf16 v[36:39], v[240:243], v[104:107], v[36:39]\n\t"
    "v_mfma_f32_16x16x32_bf16 v[36:39], v[244:247], v[108:111], v[36:39]\n\t"
    "s_waitcnt lgkmcnt(5)\n\t"
    "v_mfma_f32_16x16x32_bf16 v[40:43], v[136:139], v[56:59], 0\n\t"
    "ds_read_b128 v[84:87], v13 offset:31744\n\t"
    "v_mfma_f32_16x16x32_bf16 v[40:43], v[140:143], v[60:63], v[40:43]\n\t"
    "s_waitcnt lgkmcnt(4)\n\t"
    "v_mfma_f32_16x16x32_bf16 v[40:43], v[144:147], v[64:67], v[40:43]\n\t"
    "ds_read_b128 v[88:91], v12 offset:32768\n\t"
    "v_cvt_pk_bf16_f32 v20, v32, v33\n\t"
    "v_mfma_f32_16x16x32_bf16 v[40:43], v[148:151], v[68:71], v[40:43]\n\t"
    "s_waitcnt lgkmcnt(3)\n\t"
    "v_cvt_pk_bf16_f32 v21, v34, v35\n\t"
    "v_cvt_pk_bf16_f32 v22, v36, v37\n\t"
    "v_mfma_f32_16x16x32_bf16 v[40:43], v[152:155], v[72:75], v[40:43]\n\t"
    "v_cvt_pk_bf16_f32 v23, v38, v39\n\t"
    "v_cndmask_b32_dpp v130, v20, v22, vcc quad_perm:[1,0,3,2] row_mask:0xf bank_mask:0xf\n\t"
    "v_mfma_f32_16x16x32_bf16 v[40:43], v[156:159], v[76:79], v[40:43]\n\t"
    "s_waitcnt lgkmcnt(2)\n\t"
    "ds_read_b128 v[92:95], v13 offset:32768\n\t"
    "v_cndmask_b32_dpp v131, v21, v23, vcc quad_perm:[1,0,3,2] row_mask:0xf bank_mask:0xf\n\t"
    "v_mfma_f32_16x16x32_bf16 v[40:43], v[160:163], v[80:83], v[40:43]\n\t"
    "s_mov_b64 vcc, s[46:47]\n\t"
    "v_cndmask_b32_dpp v128, v22, v20, vcc quad_perm:[1,0,3,2] row_mask:0xf bank_mask:0xf\n\t"
    "v_cndmask_b32_dpp v129, v23, v21, vcc quad_perm:[1,0,3,2] row_mask:0xf bank_mask:0xf\n\t"
    "v_mfma_f32_16x16x32_bf16 v[44:47], v[192:195], v[56:59], 0\n\t"
    "v_permlane16_swap_b32 v128, v130\n\t"
    "v_permlane16_swap_b32 v129, v131\n\t"
    "v_mfma_f32_16x16x32_bf16 v[44:47], v[196:199], v[60:63], v[44:47]\n\t"
    "ds_read_b128 v[96:99], v12 offset:33792\n\t"
    "v_mfma_f32_16x16x32_bf16 v[44:47], v[200:203], v[64:67], v[44:47]\n\t"
    "buffer_store_dwordx4 v[128:131], v17, s[24:27], s70 offen offset:512 nt\n\t"
    "v_mfma_f32_16x16x32_bf16 v[44:47], v[204:207], v[68:71], v[44:47]\n\t"
    "v_mfma_f32_16x16x32_bf16 v[44:47], v[208:211], v[72:75], v[44:47]\n\t"
    "ds_read_b128 v[100:103], v13 offset:33792\n\t"
    "v_mfma_f32_16x16x32_bf16 v[44:47], v[212:215], v[76:79], v[44:47]\n\t"
    "v_mfma_f32_16x16x32_bf16 v[44:47], v[216:219], v[80:83], v[44:47]\n\t"
    "v_mfma_f32_16x16x32_bf16 v[48:51], a[0:3], v[56:59], 0\n\t"
    "ds_read_b128 v[104:107], v12 offset:34816\n\t"
    "v_mfma_f32_16x16x32_bf16 v[48:51], a[4:7], v[60:63], v[48:51]\n\t"
    "v_mfma_f32_16x16x32_bf16 v[48:51], a[8:11], v[64:67], v[48:51]\n\t"
    "v_mfma_f32_16x16x32_bf16 v[48:51], a[12:15], v[68:71], v[48:51]\n\t"
    "ds_read_b128 v[108:111], v13 offset:34816\n\t"
    "v_mfma_f32_16x16x32_bf16 v[48:51], a[16:19], v[72:75], v[48:51]\n\t"
    "v_mfma_f32_16x16x32_bf16 v[48:51], a[20:23], v[76:79], v[48:51]\n\t"
    "v_mfma_f32_16x16x32_bf16 v[48:51], a[24:27], v[80:83], v[48:51]\n\t"
    "v_mfma_f32_16x16x32_bf16 v[52:55], a[56:59], v[56:59], 0\n\t"
    "v_mfma_f32_16x16x32_bf16 v[52:55], a[60:63], v[60:63], v[52:55]\n\t"
    "v_mfma_f32_16x16x32_bf16 v[52:55], a[64:67], v[64:67], v[52:55]\n\t"
    "v_mfma_f32_16x16x32_bf16 v[52:55], a[68:71], v[68:71], v[52:55]\n\t"
    "v_mfma_f32_16x16x32_bf16 v[52:55], a[72:75], v[72:75], v[52:55]\n\t"
    "v_mfma_f32_16x16x32_bf16 v[52:55], a[76:79], v[76:79], v[52:55]\n\t"
    "v_mfma_f32_16x16x32_bf16 v[52:55], a[80:83], v[80:83], v[52:55]\n\t"
    "s_waitcnt lgkmcnt(5)\n\t"
    "v_mfma_f32_16x16x32_bf16 v[40:43], v[164:167], v[84:87], v[40:43]\n\t"
    "v_mfma_f32_16x16x32_bf16 v[40:43], v[168:171], v[88:91], v[40:43]\n\t"
    "s_waitcnt lgkmcnt(3)\n\t"
    "v_mfma_f32_16x16x32_bf16 v[40:43], v[172:175], v[92:95], v[40:43]\n\t"
    "v_mfma_f32_16x16x32_bf16 v[40:43], v[176:179], v[96:99], v[40:43]\n\t"
    "s_waitcnt lgkmcnt(1)\n\t"
    "v_mfma_f32_16x16x32_bf16 v[40:43], v[180:183], v[100:103], v[40:43]\n\t"
    "v_mfma_f32_16x16x32_bf16 v[40:43], v[184:187], v[104:107], v[40:43]\n\t"
    "s_waitcnt lgkmcnt(0)\n\t"
    "v_mfma_f32_16x16x32_bf16 v[40:43], v[188:191], v[108:111], v[40:43]\n\t"
    "v_mfma_f32_16x16x32_bf16 v[44:47], v[220:223], v[84:87], v[44:47]\n\t"
    "v_mfma_f32_16x16x32_bf16 v[44:47], v[224:227], v[88:91], v[44:47]\n\t"
    "v_mfma_f32_16x16x32_bf16 v[44:47], v[228:231], v[92:95], v[44:47]\n\t"
    "v_mfma_f32_16x16x32_bf16 v[44:47], v[232:235], v[96:99], v[44:47]\n\t"
    "v_mfma_f32_16x16x32_bf16 v[44:47], v[236:239], v[100:103], v[44:47]\n\t"
    "v_mfma_f32_16x16x32_bf16 v[44:47], v[240:243], v[104:107], v[44:47]\n\t"
    "v_mfma_f32_16x16x32_bf16 v[44:47], v[244:247], v[108:111], v[44:47]\n\t"
    "v_mfma_f32_16x16x32_bf16 v[48:51], a[28:31], v[84:87], v[48:51]\n\t"
    "v_mfma_f32_16x16x32_bf16 v[48:51], a[32:35], v[88:91], v[48:51]\n\t"
    "v_mfma_f32_16x16x32_bf16 v[48:51], a[36:39], v[92:95], v[48:51]\n\t"
    "v_cvt_pk_bf16_f32 v20, v40, v41\n\t"
    "v_cvt_pk_bf16_f32 v21, v42, v43\n\t"
    "v_mfma_f32_16x16x32_bf16 v[48:51], a[40:43], v[96:99], v[48:51]\n\t"
    "v_mfma_f32_16x16x32_bf16 v[48:51], a[44:47], v[100:103], v[48:51]\n\t"
    "v_mfma_f32_16x16x32_bf16 v[48:51], a[48:51], v[104:107], v[48:51]\n\t"
    "v_cvt_pk_bf16_f32 v22, v44, v45\n\t"
    "v_cvt_pk_bf16_f32 v23, v46, v47\n\t"
    "v_mfma_f32_16x16x32_bf16 v[48:51], a[52:55], v[108:111], v[48:51]\n\t"
    "v_cndmask_b32_dpp v132, v22, v20, vcc quad_perm:[1,0,3,2] row_mask:0xf bank_mask:0xf\n\t"
    "v_cndmask_b32_dpp v133, v23, v21, vcc quad_perm:[1,0,3,2] row_mask:0xf bank_mask:0xf\n\t"
    "v_mfma_f32_16x16x32_bf16 v[52:55], a[84:87], v[84:87], v[52:55]\n\t"
    "s_mov_b64 vcc, s[34:35]\n\t"
    "v_cndmask_b32_dpp v134, v20, v22, vcc quad_perm:[1,0,3,2] row_mask:0xf bank_mask:0xf\n\t"
    "v_cndmask_b32_dpp v135, v21, v23, vcc quad_perm:[1,0,3,2] row_mask:0xf bank_mask:0xf\n\t"
    "v_mfma_f32_16x16x32_bf16 v[52:55], a[88:91], v[88:91], v[52:55]\n\t"
    "v_permlane16_swap_b32 v132, v134\n\t"
    "v_permlane16_swap_b32 v133, v135\n\t"
    "v_mfma_f32_16x16x32_bf16 v[52:55], a[92:95], v[92:95], v[52:55]\n\t"
    "buffer_store_dwordx4 v[132:135], v16, s[24:27], s70 offen offset:512 nt\n\t"
    "v_mfma_f32_16x16x32_bf16 v[52:55], a[96:99], v[96:99], v[52:55]\n\t"
    "v_mfma_f32_16x16x32_bf16 v[52:55], a[100:103], v[100:103], v[52:55]\n\t"
    "v_mfma_f32_16x16x32_bf16 v[52:55], a[104:107], v[104:107], v[52:55]\n\t"
    "v_mfma_f32_16x16x32_bf16 v[52:55], a[108:111], v[108:111], v[52:55]\n\t"
    "v_cvt_pk_bf16_f32 v20, v48, v49\n\t"
    "v_cvt_pk_bf16_f32 v21, v50, v51\n\t"
    "s_nop 5\n\t"
    "v_cvt_pk_bf16_f32 v22, v52, v53\n\t"
    "v_cvt_pk_bf16_f32 v23, v54, v55\n\t"
    "s_nop 0\n\t"
    "v_cndmask_b32_dpp v250, v20, v22, vcc quad_perm:[1,0,3,2] row_mask:0xf bank_mask:0xf\n\t"
    "v_cndmask_b32_dpp v251, v21, v23, vcc quad_perm:[1,0,3,2] row_mask:0xf bank_mask:0xf\n\t"
    "s_mov_b64 vcc, s[46:47]\n\t"
    "v_cndmask_b32_dpp v248, v22, v20, vcc quad_perm:[1,0,3,2] row_mask:0xf bank_mask:0xf\n\t"
    "v_cndmask_b32_dpp v249, v23, v21, vcc quad_perm:[1,0,3,2] row_mask:0xf bank_mask:0xf\n\t"
    "s_nop 0\n\t"
    "v_permlane16_swap_b32 v248, v250\n\t"
    "v_permlane16_swap_b32 v249, v251\n\t"
    "buffer_store_dwordx4 v[248:251], v18, s[24:27], s70 offen nt\n\t"
    "Lend_%=:\n\t"
    :: "s"(__builtin_amdgcn_kernarg_segment_ptr()), "s"(blockIdx.x), "v"(tid), "s"((unsigned)(size_t)smem)
    : "v1", "v2", "v3", "v4", "v5", "v6", "v7", "v8", "v9", "v10", "v11", "v12", "v13", "v14", "v15", "v16", "v17", "v18", "v19", "v20", "v21", "v22", "v23", "v24", "v25", "v26", "v27", "v28", "v29", "v30", "v31", "v32", "v33", "v34", "v35", "v36", "v37", "v38", "v39", "v40", "v41", "v42", "v43", "v44", "v45", "v46", "v47", "v48", "v49", "v50", "v51", "v52", "v53", "v54", "v55", "v56", "v57", "v58", "v59", "v60", "v61", "v62", "v63", "v64", "v65", "v66", "v67", "v68", "v69", "v70", "v71", "v72", "v73", "v74", "v75", "v76", "v77", "v78", "v79", "v80", "v81", "v82", "v83", "v84", "v85", "v86", "v87", "v88", "v89", "v90", "v91", "v92", "v93", "v94", "v95", "v96", "v97", "v98", "v99", "v100", "v101", "v102", "v103", "v104", "v105", "v106", "v107", "v108", "v109", "v110", "v111", "v112", "v113", "v114", "v115", "v116", "v117", "v118", "v119", "v120", "v121", "v122", "v123", "v124", "v125", "v126", "v127", "v128", "v129", "v130", "v131", "v132", "v133", "v134", "v135", "v136", "v137", "v138", "v139", "v140", "v141", "v142", "v143", "v144", "v145", "v146", "v147", "v148", "v149", "v150", "v151", "v152", "v153", "v154", "v155", "v156", "v157", "v158", "v159", "v160", "v161", "v162", "v163", "v164", "v165", "v166", "v167", "v168", "v169", "v170", "v171", "v172", "v173", "v174", "v175", "v176", "v177", "v178", "v179", "v180", "v181", "v182", "v183", "v184", "v185", "v186", "v187", "v188", "v189", "v190", "v191", "v192", "v193", "v194", "v195", "v196", "v197", "v198", "v199", "v200", "v201", "v202", "v203", "v204", "v205", "v206", "v207", "v208", "v209", "v210", "v211", "v212", "v213", "v214", "v215", "v216", "v217", "v218", "v219", "v220", "v221", "v222", "v223", "v224", "v225", "v226", "v227", "v228", "v229", "v230", "v231", "v232", "v233", "v234", "v235", "v236", "v237", "v238", "v239", "v240", "v241", "v242", "v243", "v244", "v245", "v246", "v247", "v248", "v249", "v250", "v251", "v252", "v253", "v254", "v255", "a0", "a1", "a2", "a3", "a4", "a5", "a6", "a7", "a8", "a9", "a10", "a11", "a12", "a13", "a14", "a15", "a16", "a17", "a18", "a19", "a20", "a21", "a22", "a23", "a24", "a25", "a26", "a27", "a28", "a29", "a30", "a31", "a32", "a33", "a34", "a35", "a36", "a37", "a38", "a39", "a40", "a41", "a42", "a43", "a44", "a45", "a46", "a47", "a48", "a49", "a50", "a51", "a52", "a53", "a54", "a55", "a56", "a57", "a58", "a59", "a60", "a61", "a62", "a63", "a64", "a65", "a66", "a67", "a68", "a69", "a70", "a71", "a72", "a73", "a74", "a75", "a76", "a77", "a78", "a79", "a80", "a81", "a82", "a83", "a84", "a85", "a86", "a87", "a88", "a89", "a90", "a91", "a92", "a93", "a94", "a95", "a96", "a97", "a98", "a99", "a100", "a101", "a102", "a103", "a104", "a105", "a106", "a107", "a108", "a109", "a110", "a111", "a112", "a113", "a114", "a115", "a116", "a117", "a118", "a119", "a120", "a121", "a122", "a123", "a124", "a125", "a126", "a127", "a128", "a129", "a130", "a131", "a132", "a133", "a134", "a135", "a136", "a137", "a138", "a139", "a140", "a141", "a142", "a143", "a144", "a145", "a146", "a147", "a148", "a149", "a150", "a151", "a152", "a153", "a154", "a155", "a156", "a157", "a158", "a159", "a160", "a161", "a162", "a163", "a164", "a165", "a166", "a167", "a168", "a169", "a170", "a171", "a172", "a173", "a174", "a175", "a176", "a177", "a178", "a179", "a180", "a181", "a182", "a183", "a184", "a185", "a186", "a187", "a188", "a189", "a190", "a191", "a192", "a193", "a194", "a195", "a196", "a197", "a198", "a199", "a200", "a201", "a202", "a203", "a204", "a205", "a206", "a207", "a208", "a209", "a210", "a211", "a212", "a213", "a214", "a215", "a216", "a217", "a218", "a219", "a220", "a221", "a222", "a223", "a224", "a225", "a226", "a227", "a228", "a229", "a230", "a231", "a232", "a233", "a234", "a235", "a236", "a237", "a238", "a239", "a240", "a241", "a242", "a243", "a244", "a245", "a246", "a247", "a248", "a249", "a250", "a251", "a252", "a253", "a254", "a255", "s4", "s5", "s6", "s7", "s8", "s9", "s10", "s11", "s12", "s13", "s14", "s15", "s16", "s17", "s18", "s19", "s20", "s21", "s22", "s23", "s24", "s25", "s26", "s27", "s28", "s29", "s30", "s31", "s34", "s35", "s36", "s37", "s38", "s39", "s40", "s41", "s42", "s43", "s44", "s45", "s46", "s47", "s48", "s49", "s50", "s51", "s52", "s53", "s54", "s55", "s56", "s57", "s58", "s59", "s60", "s61", "s62", "s63", "s64", "s65", "s66", "s67", "s68", "s69", "s70", "s71", "s72", "s73", "s74", "s75", "s76", "s77", "s78", "s79", "s80", "s81", "s82", "s83", "s84", "s85", "s86", "s87", "s88", "s89", "s90", "s91", "s92", "s93", "s94", "s95", "vcc", "scc", "memory");
}

extern "C" void kernel_launch(void* const* d_in, const int* in_sizes, int n_in,
                              void* d_out, int out_size, void* d_ws, size_t ws_size,
                              hipStream_t stream) {
    int M = in_sizes[0] / 448;
    ln_linear_kernel<<<M / 128, 256, 0, stream>>>(d_in[0], d_in[1], d_in[2], d_in[3], d_out);
}
```
